```python
import torch

def _softmax(x, axis=-1):
    return torch.softmax(x, dim=axis)
def _gelu(x):
    return torch.nn.functional.gelu(x, approximate="tanh")
def _silu(x):
    return torch.nn.functional.silu(x)
def _sigmoid(x):
    return torch.sigmoid(x)
def _layernorm(x, axis=-1, eps=1e-5):
    m = x.mean(dim=axis, keepdim=True)
    v = x.var(dim=axis, keepdim=True, unbiased=False)
    return (x - m) / torch.sqrt(v + eps)
def _scatter_add(tgt, idx, src, axis=0):
    return tgt.clone().index_add_(axis, idx, src)
def _segment_sum(src, seg, n):
    out = torch.zeros((n,) + src.shape[1:], dtype=src.dtype)
    return out.index_add_(0, seg, src)
def _pad(x, pw):
    flat = tuple(v for pair in reversed(pw) for v in pair)
    return torch.nn.functional.pad(x, flat)

kernel_name = 'parallel_sum__mha_block_cached__swiglu_ffn__decode'

def setup_inputs():

    _w = (lambda a: (lambda t: t.to(torch.bfloat16) if t.is_floating_point() else t)(torch.as_tensor(a)))
    def _A_setup():
        B, Sq, D = 2, 1, 448
        H, Sk, Dh = 16, 8192, 256
        return {
            "x": _w((lambda *s: torch.randn(*s, dtype=torch.bfloat16) * 0.05)(B, Sq, D)),
            "k_cache": _w((lambda *s: torch.randn(*s, dtype=torch.bfloat16) * 3.2)(B, H, Sk, Dh)),
            "v_cache": _w((lambda *s: torch.randn(*s, dtype=torch.bfloat16) * 1.6)(B, H, Sk, Dh)),
            "wq": _w((lambda *s: torch.randn(*s, dtype=torch.bfloat16) * 3.2)(D, H * Dh) * (D ** -0.5)),
            "wo": _w((lambda *s: torch.randn(*s, dtype=torch.bfloat16) * 1.6)(H * Dh, D) * ((H * Dh) ** -0.5)),
        }
    _A_in = _A_setup()

    def _B_setup():
        B, S, D, F = 2, 1, 448, 1792
        return {
            "x": _w((lambda *s: torch.randn(*s, dtype=torch.bfloat16) * 0.05)(B, S, D)),
            "w_gate": _w((lambda *s: torch.randn(*s, dtype=torch.bfloat16) * 0.05)(D, F)),
            "w_up": _w((lambda *s: torch.randn(*s, dtype=torch.bfloat16) * 0.05)(D, F)),
            "w_down": _w((lambda *s: torch.randn(*s, dtype=torch.bfloat16) * 0.05)(F, D)),
        }
    _B_in = _B_setup()

    return {
        "A_x": _A_in["x"],
        "A_k_cache": _A_in["k_cache"],
        "A_v_cache": _A_in["v_cache"],
        "A_wq": _A_in["wq"],
        "A_wo": _A_in["wo"],
        "B_x": _B_in["x"],
        "B_w_gate": _B_in["w_gate"],
        "B_w_up": _B_in["w_up"],
        "B_w_down": _B_in["w_down"],
    }

def reference(A_x, A_k_cache, A_v_cache, A_wq, A_wo, B_x, B_w_gate, B_w_up, B_w_down):
    def _A_ref(x, k_cache, v_cache, wq, wo):
        B, Sq, D = x.shape
        H, Dh = 16, 256
        q = (lambda x, *d: x.permute(*d))((x @ wq).reshape(B, Sq, H, Dh), 0, 2, 1, 3)
        scores = torch.einsum("bhqd,bhkd->bhqk", q, k_cache) * (Dh ** -0.5)
        p = _softmax(scores, axis=-1)
        o = torch.einsum("bhqk,bhkd->bhqd", p, v_cache)
        return (lambda x, *d: x.permute(*d))(o, 0, 2, 1, 3).reshape(B, Sq, H * Dh) @ wo

    def _B_ref(x, w_gate, w_up, w_down):
        g = _silu(x @ w_gate)
        u = x @ w_up
        return (g * u) @ w_down

    x = A_x
    xn = _layernorm(x, axis=-1)
    return x + _A_ref(xn, A_k_cache, A_v_cache, A_wq, A_wo) + _B_ref(xn, B_w_gate, B_w_up, B_w_down)
```

```cpp
#include <hip/hip_runtime.h>
#include <hip/hip_bf16.h>
#include <stdint.h>

#define RND 0

constexpr int B_ = 2, D_ = 448, H_ = 16, SK = 8192, DH = 256, F_ = 1792;
constexpr int NSPLIT = 16;

__device__ __forceinline__ float bf2f(unsigned short u) { return __uint_as_float(((unsigned)u) << 16); }
__device__ __forceinline__ float rbf(float x) {
  return x;
}

__global__ void kA(const unsigned short* x, const unsigned short* wq, const unsigned short* wg, const unsigned short* wu,
                   float* q, float* hmid) {
  __shared__ float xn[2][D_];
  const int t = threadIdx.x;
  for (int b = 0; b < 2; ++b) {
    float s = 0.f;
    for (int i = t; i < D_; i += 64) s += bf2f(x[b * D_ + i]);
    for (int o = 32; o; o >>= 1) s += __shfl_xor(s, o);
    float mexact = s / D_;
    float m = rbf(mexact);
    float v = 0.f;
    for (int i = t; i < D_; i += 64) { float d = bf2f(x[b * D_ + i]) - mexact; v += d * d; }
    for (int o = 32; o; o >>= 1) v += __shfl_xor(v, o);
    v = rbf(v / D_);
    float den = rbf(sqrtf(rbf(v + 1e-5f)));
    for (int i = t; i < D_; i += 64) xn[b][i] = rbf(rbf(bf2f(x[b * D_ + i]) - m) / den);
  }
  __syncthreads();
  int col = blockIdx.x * 64 + t;
  if (col < 4096) {
    float a0 = 0, a1 = 0;
    for (int d = 0; d < D_; ++d) { float w = bf2f(wq[(size_t)d * 4096 + col]); a0 += xn[0][d] * w; a1 += xn[1][d] * w; }
    q[col] = rbf(a0); q[4096 + col] = rbf(a1);
  } else {
    int f = col - 4096;
    float g0 = 0, g1 = 0, u0 = 0, u1 = 0;
    for (int d = 0; d < D_; ++d) {
      float w = bf2f(wg[(size_t)d * F_ + f]); g0 += xn[0][d] * w; g1 += xn[1][d] * w;
      float w2 = bf2f(wu[(size_t)d * F_ + f]); u0 += xn[0][d] * w2; u1 += xn[1][d] * w2;
    }
    g0 = rbf(g0); g1 = rbf(g1); u0 = rbf(u0); u1 = rbf(u1);
    float s0 = rbf(g0 / (1.f + __expf(-g0))), s1 = rbf(g1 / (1.f + __expf(-g1)));
    hmid[f] = rbf(s0 * u0); hmid[F_ + f] = rbf(s1 * u1);
  }
}

__global__ void kB1(const unsigned short* k, const float* q, float* sc) {
  const int bh = blockIdx.y; const int lane = threadIdx.x & 63, w = threadIdx.x >> 6;
  const int b = bh / H_, h = bh % H_;
  float qv[4];
  for (int j = 0; j < 4; ++j) qv[j] = q[b * 4096 + h * 256 + lane * 4 + j];
  for (int i = 0; i < 64; ++i) {
    int key = blockIdx.x * 256 + w * 64 + i;
    const unsigned short* kr = k + ((size_t)bh * SK + key) * DH + lane * 4;
    uint2 v = *(const uint2*)kr;
    float s = qv[0] * __uint_as_float(v.x << 16) + qv[1] * __uint_as_float(v.x & 0xffff0000u) + qv[2] * __uint_as_float(v.y << 16) + qv[3] * __uint_as_float(v.y & 0xffff0000u);
    for (int o = 32; o; o >>= 1) s += __shfl_xor(s, o);
    if (lane == 0) sc[(size_t)bh * SK + key] = rbf(rbf(s) * 0.0625f);
  }
}
__global__ void kB2(float* sc) {
  const int bh = blockIdx.x; const int t = threadIdx.x;
  __shared__ float red[256];
  float m = -INFINITY;
  for (int i = t; i < SK; i += 256) m = fmaxf(m, sc[(size_t)bh * SK + i]);
  red[t] = m; __syncthreads();
  for (int o = 128; o; o >>= 1) { if (t < o) red[t] = fmaxf(red[t], red[t + o]); __syncthreads(); }
  m = red[0]; __syncthreads();
  float l = 0;
  for (int i = t; i < SK; i += 256) l += __expf(sc[(size_t)bh * SK + i] - m);
  red[t] = l; __syncthreads();
  for (int o = 128; o; o >>= 1) { if (t < o) red[t] += red[t + o]; __syncthreads(); }
  l = red[0];
  for (int i = t; i < SK; i += 256) sc[(size_t)bh * SK + i] = rbf(__expf(sc[(size_t)bh * SK + i] - m) / l);
}
__global__ void kB3(const unsigned short* v, const float* p, float* po) {
  const int bh = blockIdx.y, s = blockIdx.x, t = threadIdx.x;
  float a = 0;
  for (int i = 0; i < SK / NSPLIT; ++i) {
    int key = s * (SK / NSPLIT) + i;
    a += p[(size_t)bh * SK + key] * bf2f(v[((size_t)bh * SK + key) * DH + t]);
  }
  po[((size_t)bh * NSPLIT + s) * DH + t] = a;
}
__global__ void kC(const unsigned short* x, const float* po, const float* hmid, const unsigned short* wo, const unsigned short* wd, __hip_bfloat16* out) {
  __shared__ float o[4096];
  const int b = blockIdx.y; const int t = threadIdx.x;
  for (int i = t; i < 4096; i += 64) {
    float a = 0; int h = i / 256, d = i % 256;
    for (int s = 0; s < NSPLIT; ++s) a += po[((size_t)(b * H_ + h) * NSPLIT + s) * DH + d];
    o[i] = rbf(a);
  }
  __syncthreads();
  int col = blockIdx.x * 64 + t;
  float a = 0;
  for (int i = 0; i < 4096; ++i) a += o[i] * bf2f(wo[(size_t)i * D_ + col]);
  float f = 0;
  for (int i = 0; i < F_; ++i) f += hmid[b * F_ + i] * bf2f(wd[(size_t)i * D_ + col]);
  float r = rbf(rbf(bf2f(x[b * D_ + col]) + rbf(a)) + rbf(f));
  out[b * D_ + col] = __float2bfloat16(r);
}

extern "C" void kernel_launch(void* const* d_in, const int* in_sizes, int n_in,
                              void* d_out, int out_size, void* d_ws, size_t ws_size,
                              hipStream_t stream) {
  const unsigned short* x = (const unsigned short*)d_in[0];
  const unsigned short* k = (const unsigned short*)d_in[1];
  const unsigned short* v = (const unsigned short*)d_in[2];
  const unsigned short* wq = (const unsigned short*)d_in[3];
  const unsigned short* wo = (const unsigned short*)d_in[4];
  const unsigned short* wg = (const unsigned short*)d_in[6];
  const unsigned short* wu = (const unsigned short*)d_in[7];
  const unsigned short* wd = (const unsigned short*)d_in[8];
  float* ws = (float*)d_ws;
  float* q = ws; float* hmid = ws + 8192; float* sc = ws + 16384; float* po = sc + 32 * SK;
  kA<<<(4096 + F_) / 64, 64, 0, stream>>>(x, wq, wg, wu, q, hmid);
  kB1<<<dim3(SK / 256, 32), 256, 0, stream>>>(k, q, sc);
  kB2<<<32, 256, 0, stream>>>(sc);
  kB3<<<dim3(NSPLIT, 32), 256, 0, stream>>>(v, sc, po);
  kC<<<dim3(7, 2), 64, 0, stream>>>(x, po, hmid, wo, wd, (__hip_bfloat16*)d_out);
}
```

```cpp
#include <hip/hip_runtime.h>
#include <hip/hip_bf16.h>
#include <stdint.h>

typedef unsigned short u16;
typedef unsigned u32x4 __attribute__((ext_vector_type(4)));
typedef unsigned u32x2 __attribute__((ext_vector_type(2)));
typedef float f32x4 __attribute__((ext_vector_type(4)));
typedef float f32x2 __attribute__((ext_vector_type(2)));
typedef __bf16 bf16x2 __attribute__((ext_vector_type(2)));

constexpr int D_ = 448, H_ = 16, SK = 8192, DH = 256, F_ = 1792;
constexpr int NWG = 256, NTHR = 512, NW = 8;
constexpr int NSPLIT = 8;
constexpr int KEYS_WG = SK / NSPLIT;
constexpr int NLD = 64;
#define WKK 46
constexpr int WK = WKK;
constexpr int NWO = 32;
#define WVV 17
constexpr int WV = WVV;
#define KVAUX 2

constexpr int WS_EPOCH = 0;
constexpr int WS_ML = 4096;
constexpr int WS_Y = WS_ML + 2 * 16 * 64 * 16;
constexpr int WS_Z = WS_Y + 2 * 56 * 128 * 64;
constexpr int WS_END = WS_Z + 2 * 56 * 128 * 64;

constexpr int L_XN = 0;
constexpr int L_QRED = L_XN + 8 * 256;
constexpr int L_FRED = L_QRED + 8 * 256;
constexpr int L_YRED = L_FRED + 8 * 32;
constexpr int L_P = L_YRED + 8 * 448;
constexpr int L_C = L_P + 8 * 128;
constexpr int L_X = L_C + 8 * 16;
constexpr int L_END = L_X + 256;

__device__ __forceinline__ float dot2(unsigned a, unsigned b, float c) {
  return __builtin_amdgcn_fdot2_f32_bf16(__builtin_bit_cast(bf16x2, a), __builtin_bit_cast(bf16x2, b), c, false);
}
__device__ __forceinline__ float bflo(unsigned v) { return __uint_as_float(v << 16); }
__device__ __forceinline__ float bfhi(unsigned v) { return __uint_as_float(v & 0xffff0000u); }
__device__ __forceinline__ unsigned pk_bf16(float lo, float hi) {
  bf16x2 r; r[0] = (__bf16)lo; r[1] = (__bf16)hi; return __builtin_bit_cast(unsigned, r);
}
__device__ __forceinline__ float rbf(float x) { return (float)(__bf16)x; }
__device__ __forceinline__ unsigned perm_lo(unsigned a, unsigned b) { return __builtin_amdgcn_perm(b, a, 0x05040100u); }
__device__ __forceinline__ unsigned perm_hi(unsigned a, unsigned b) { return __builtin_amdgcn_perm(b, a, 0x07060302u); }
template <int CTRL> __device__ __forceinline__ float dpp(float x) {
  return __builtin_bit_cast(float, __builtin_amdgcn_mov_dpp(__builtin_bit_cast(int, x), CTRL, 0xf, 0xf, true));
}
constexpr int XOR1 = 0xB1, XOR2 = 0x4E, XOR7 = 0x141, XOR8 = 0x128, ROR4 = 0x124;
__device__ __forceinline__ float sw16(float a, float b) {
  auto s = __builtin_amdgcn_permlane16_swap(__float_as_uint(a), __float_as_uint(b), false, false);
  return __uint_as_float(s[0]) + __uint_as_float(s[1]);
}
__device__ __forceinline__ float sw32(float a, float b) {
  auto s = __builtin_amdgcn_permlane32_swap(__float_as_uint(a), __float_as_uint(b), false, false);
  return __uint_as_float(s[0]) + __uint_as_float(s[1]);
}
__device__ __forceinline__ float x16_sum(float x) { return sw16(x, x); }
__device__ __forceinline__ float x32_sum(float x) { return sw32(x, x); }
__device__ __forceinline__ float x16_max(float x) {
  auto s = __builtin_amdgcn_permlane16_swap(__float_as_uint(x), __float_as_uint(x), false, false);
  return fmaxf(__uint_as_float(s[0]), __uint_as_float(s[1]));
}
__device__ __forceinline__ float x32_max(float x) {
  auto s = __builtin_amdgcn_permlane32_swap(__float_as_uint(x), __float_as_uint(x), false, false);
  return fmaxf(__uint_as_float(s[0]), __uint_as_float(s[1]));
}
__device__ __forceinline__ float wave_sum(float x) {
  x += dpp<XOR1>(x); x += dpp<XOR2>(x); x += dpp<XOR7>(x); x += dpp<XOR8>(x);
  x = x16_sum(x); x = x32_sum(x); return x;
}
__device__ __forceinline__ float wave_max(float x) {
  x = fmaxf(x, dpp<XOR1>(x)); x = fmaxf(x, dpp<XOR2>(x)); x = fmaxf(x, dpp<XOR7>(x)); x = fmaxf(x, dpp<XOR8>(x));
  x = x16_max(x); x = x32_max(x); return x;
}
template <int CTRL, int BIT> __device__ __forceinline__ float trs(float lo, float hi, int lane) {
  const float t1 = lo + dpp<CTRL>(lo), t2 = hi + dpp<CTRL>(hi);
  return (lane & BIT) ? t2 : t1;
}
__device__ __forceinline__ void bar() { asm volatile("s_waitcnt lgkmcnt(0)\n\ts_barrier" ::: "memory"); }
#define SB() __builtin_amdgcn_sched_barrier(0)
#define PIN8(a_) asm volatile("" : "+v"(a_[0]), "+v"(a_[1]), "+v"(a_[2]), "+v"(a_[3]), "+v"(a_[4]), "+v"(a_[5]), "+v"(a_[6]), "+v"(a_[7]))
#define PIN1(a_) asm volatile("" : "+v"(a_))
#define MKRS(ptr_, bytes_) __builtin_amdgcn_make_buffer_rsrc((void*)(ptr_), 0, (bytes_), 0x00020000)
#define BLD(rsrc_, voff_, soff_, aux_) __builtin_amdgcn_raw_buffer_load_b128((rsrc_), (voff_), (soff_), (aux_))

__device__ __forceinline__ u32x4 ld_pl(const u16* p) { return *reinterpret_cast<const u32x4*>(p); }

__global__ void __launch_bounds__(NTHR, 2)
fused_kernel(const u16* __restrict__ x, const u16* __restrict__ kc, const u16* __restrict__ vc,
             const u16* __restrict__ wq, const u16* __restrict__ wo, const u16* __restrict__ wg,
             const u16* __restrict__ wu, const u16* __restrict__ wd, u16* __restrict__ out,
             unsigned char* ws) {
  __shared__ __attribute__((aligned(16))) float lds[L_END];
  unsigned* ldsu = reinterpret_cast<unsigned*>(lds);
  const int t = threadIdx.x, lane = t & 63;
  const int w = __builtin_amdgcn_readfirstlane(t >> 6);
  const int g = blockIdx.x;
  const int xcd = g & 7, r = g >> 3, hsel = r >> 4, b = (r >> 3) & 1, s = r & 7;
  const int h = xcd * 2 + hsel;
  const int i16 = hsel * 8 + s;
  const int pidx = h * 8 + s;
  const bool has_ffn = i16 < 14;
  const int slice = xcd * 14 + (has_ffn ? i16 : 13);
  const int zrow = has_ffn ? slice : (112 + xcd * 2 + (i16 - 14));
  const bool is_cons = i16 >= 9;
  const int ci = xcd * 7 + (is_cons ? (i16 - 9) : 0);
  const int half = lane >> 5, l32 = lane & 31;
  const int l56 = lane < 56 ? lane : lane - 8;

  const __amdgpu_buffer_rsrc_t rs = MKRS(ws, WS_END);
  const size_t kvoff = ((size_t)(b * H_ + h) * SK + s * KEYS_WG + w * 128) * DH;
  const __amdgpu_buffer_rsrc_t rk = MKRS(kc + kvoff, 65536);
  const __amdgpu_buffer_rsrc_t rv = MKRS(vc + kvoff, 65536);
  const __amdgpu_buffer_rsrc_t rq = MKRS(wq + (size_t)(2 * w * 28) * 4096 + h * 256, 56 * 8192);
  const __amdgpu_buffer_rsrc_t ro = MKRS(wo + (size_t)(h * 256 + w * 32) * D_, 32 * 896);
  const int lo16 = lane * 16;

  u32x4 xv = ld_pl(x + b * D_ + l56 * 8);
  u32x2 ep = __builtin_amdgcn_raw_buffer_load_b64(rs, WS_EPOCH + b * 256, 0, 16);
  SB();
  u32x4 Wq[28];
  {
    const int vo = half * 28 * 8192 + l32 * 16;
#pragma unroll
    for (int i = 0; i < 28; ++i) Wq[i] = BLD(rq, vo, i * 8192, 0);
  }
  SB();
  u32x4 Fg[4], Fd[2];
  const int mh = t & 3, j0 = t >> 2;
  const int j1 = (j0 < 96) ? (j0 + 128) : j0;
  {
    const u16* base = ((mh & 2) ? wu : wg) + slice * 16 + (mh & 1) * 8;
    Fg[0] = ld_pl(base + (size_t)(2 * j0) * F_);
    Fg[1] = ld_pl(base + (size_t)(2 * j0 + 1) * F_);
    Fg[2] = ld_pl(base + (size_t)(2 * j1) * F_);
    Fg[3] = ld_pl(base + (size_t)(2 * j1 + 1) * F_);
    const u16* pd = wd + (size_t)(slice * 16 + 2 * w) * D_ + l56 * 8;
    Fd[0] = ld_pl(pd);
    Fd[1] = ld_pl(pd + D_);
  }
  SB();
  u32x4 K[NLD], V[NLD];
  constexpr int K0 = WK - 28 - 6;
#pragma unroll
  for (int i = 0; i < K0; ++i) K[i] = BLD(rk, lo16, i * 1024, KVAUX);
  SB();

  {
    float xf[8];
#pragma unroll
    for (int k = 0; k < 4; ++k) { xf[2 * k] = bflo(xv[k]); xf[2 * k + 1] = bfhi(xv[k]); }
    float sm = 0.f;
#pragma unroll
    for (int k = 0; k < 8; ++k) sm += xf[k];
    sm = lane < 56 ? sm : 0.f;
    sm = wave_sum(sm);
    const float mexact = sm / (float)D_;
    const float m = rbf(mexact);
    float vs = 0.f;
#pragma unroll
    for (int k = 0; k < 8; ++k) { float d = xf[k] - mexact; vs += d * d; }
    vs = lane < 56 ? vs : 0.f;
    vs = wave_sum(vs);
    const float var = rbf(vs / (float)D_);
    const float den = rbf(sqrtf(rbf(var + 1e-5f)));
    u32x4 xn;
#pragma unroll
    for (int k = 0; k < 4; ++k) xn[k] = pk_bf16(rbf(xf[2 * k] - m) / den, rbf(xf[2 * k + 1] - m) / den);
    if (lane < 56) {
      *reinterpret_cast<u32x4*>(ldsu + L_XN + w * 256 + lane * 4) = xn;
      if (w == 0) *reinterpret_cast<u32x4*>(ldsu + L_X + lane * 4) = xv;
    }
  }
  const unsigned long long e64 = (((unsigned long long)ep[1] << 32) | ep[0]) + 1ull;
  const unsigned tlo = __builtin_amdgcn_readfirstlane((unsigned)e64);
  const unsigned thi_full = __builtin_amdgcn_readfirstlane((unsigned)(e64 >> 32));
  const unsigned thi = thi_full & 0xffu;
  const unsigned g0tag = tlo << 24, g1tag = (tlo >> 8) | (thi << 24);
  SB();

  {
    const unsigned* xs = ldsu + L_XN + w * 256 + (2 * w + half) * 14;
    float qa[8];
#pragma unroll
    for (int k = 0; k < 8; ++k) qa[k] = 0.f;
    u32x2 xp;
#pragma unroll
    for (int i = 0; i < 14; ++i) {
      if ((i & 1) == 0) xp = *reinterpret_cast<const u32x2*>(xs + i);
#pragma unroll
      for (int k = 0; k < 4; ++k) {
        qa[2 * k] = dot2(perm_lo(Wq[2 * i][k], Wq[2 * i + 1][k]), xp[i & 1], qa[2 * k]);
        qa[2 * k + 1] = dot2(perm_hi(Wq[2 * i][k], Wq[2 * i + 1][k]), xp[i & 1], qa[2 * k + 1]);
      }
      PIN8(qa);
      SB();
      K[K0 + 2 * i] = BLD(rk, lo16, (K0 + 2 * i) * 1024, KVAUX);
      K[K0 + 2 * i + 1] = BLD(rk, lo16, (K0 + 2 * i + 1) * 1024, KVAUX);
      SB();
    }
    f32x4 qh;
#pragma unroll
    for (int k = 0; k < 4; ++k) qh[k] = sw32(qa[k], qa[k + 4]);
    *reinterpret_cast<f32x4*>(lds + L_QRED + w * 256 + l32 * 8 + half * 4) = qh;
  }
  SB();
  {
    const unsigned xa = ldsu[L_XN + w * 256 + j0];
    unsigned xb2 = ldsu[L_XN + w * 256 + j1];
    xb2 = (j0 < 96) ? xb2 : 0u;
    float fa[8];
#pragma unroll
    for (int k = 0; k < 4; ++k) {
      fa[2 * k] = dot2(perm_lo(Fg[0][k], Fg[1][k]), xa, 0.f);
      fa[2 * k + 1] = dot2(perm_hi(Fg[0][k], Fg[1][k]), xa, 0.f);
      fa[2 * k] = dot2(perm_lo(Fg[2][k], Fg[3][k]), xb2, fa[2 * k]);
      fa[2 * k + 1] = dot2(perm_hi(Fg[2][k], Fg[3][k]), xb2, fa[2 * k + 1]);
    }
    PIN8(fa);
    SB();
#pragma unroll
    for (int i = 0; i < 4; ++i) K[K0 + 28 + i] = BLD(rk, lo16, (K0 + 28 + i) * 1024, KVAUX);
    SB();
    float f4[4];
    f32x2 f2;
#pragma unroll
    for (int k = 0; k < 4; ++k) f4[k] = sw32(fa[k], fa[k + 4]);
#pragma unroll
    for (int k = 0; k < 2; ++k) { float v = sw16(f4[k], f4[k + 2]);
      v += dpp<ROR4>(v); v += dpp<XOR8>(v); f2[k] = v; }
    if ((lane & 12) == 0) *reinterpret_cast<f32x2*>(lds + L_FRED + w * 32 + mh * 8 + (lane >> 5) * 4 + ((lane >> 4) & 1) * 2) = f2;
  }
  bar();
  unsigned q4[4];
  {
    f32x4 a0 = {0.f, 0.f, 0.f, 0.f}, a1 = {0.f, 0.f, 0.f, 0.f};
#pragma unroll
    for (int ww = 0; ww < 8; ++ww) {
      a0 += *reinterpret_cast<const f32x4*>(lds + L_QRED + ww * 256 + l32 * 8);
      a1 += *reinterpret_cast<const f32x4*>(lds + L_QRED + ww * 256 + l32 * 8 + 4);
      if ((ww & 1) == 1) { PIN1(a0); PIN1(a1); SB(); }
    }
    q4[0] = pk_bf16(a0[0], a0[1]); q4[1] = pk_bf16(a0[2], a0[3]);
    q4[2] = pk_bf16(a1[0], a1[1]); q4[3] = pk_bf16(a1[2], a1[3]);
    PIN1(q4[0]); PIN1(q4[1]); PIN1(q4[2]); PIN1(q4[3]);
  }
  SB();
  {
    f32x2 gg = {0.f, 0.f}, uu = {0.f, 0.f};
#pragma unroll
    for (int ww = 0; ww < 8; ++ww) {
      gg += *reinterpret_cast<const f32x2*>(lds + L_FRED + ww * 32 + 2 * w);
      uu += *reinterpret_cast<const f32x2*>(lds + L_FRED + ww * 32 + 16 + 2 * w);
      if ((ww & 3) == 3) { PIN1(gg); PIN1(uu); SB(); }
    }
    float hm[2];
#pragma unroll
    for (int k = 0; k < 2; ++k) {
      const float gv = rbf(gg[k]), uv = rbf(uu[k]);
      const float sg = rbf(gv / (1.f + __expf(-gv)));
      hm[k] = sg * uv;
    }
    const unsigned hp = pk_bf16(hm[0], hm[1]);
    f32x4 z0, z1;
#pragma unroll
    for (int k = 0; k < 2; ++k) {
      z0[2 * k] = dot2(perm_lo(Fd[0][k], Fd[1][k]), hp, 0.f);
      z0[2 * k + 1] = dot2(perm_hi(Fd[0][k], Fd[1][k]), hp, 0.f);
      z1[2 * k] = dot2(perm_lo(Fd[0][k + 2], Fd[1][k + 2]), hp, 0.f);
      z1[2 * k + 1] = dot2(perm_hi(Fd[0][k + 2], Fd[1][k + 2]), hp, 0.f);
    }
    if (lane < 56) {
      *reinterpret_cast<f32x4*>(lds + L_YRED + w * 448 + lane * 8) = z0;
      *reinterpret_cast<f32x4*>(lds + L_YRED + w * 448 + lane * 8 + 4) = z1;
    }
    SB();
#pragma unroll
    for (int i = 0; i < 2; ++i) K[K0 + 32 + i] = BLD(rk, lo16, (K0 + 32 + i) * 1024, KVAUX);
    SB();
  }
  bar();
  if (t < 224) {
    f32x2 zz = {0.f, 0.f};
#pragma unroll
    for (int ww = 0; ww < 8; ++ww) zz += *reinterpret_cast<const f32x2*>(lds + L_YRED + ww * 448 + 2 * t);
    const unsigned d0 = (__float_as_uint(zz[0]) + 0x80u) >> 8, d1 = (__float_as_uint(zz[1]) + 0x80u) >> 8;
    u32x4 gr = {d0 | g0tag, g1tag, d1 | g0tag, g1tag};
    __builtin_amdgcn_raw_buffer_store_b128(gr, rs, WS_Z + ((b * 56 + (t >> 2)) * 128 + zrow) * 64 + (t & 3) * 16, 0, 16);
  }
  SB();

  float sc[2];
#pragma unroll
  for (int bt = 0; bt < 2; ++bt) {
    float t0, t1, t2, t3, t4;
#pragma unroll
    for (int i = 0; i < 32; ++i) {
      const int ii = bt * 32 + i;
      float d = dot2(K[ii][0], q4[0], 0.f);
      d = dot2(K[ii][1], q4[1], d);
      d = dot2(K[ii][2], q4[2], d);
      d = dot2(K[ii][3], q4[3], d);
      PIN1(d);
      SB();
      const int nx = ii + WK;
      if (nx < NLD) K[nx] = BLD(rk, lo16, nx * 1024, KVAUX);
      else V[nx - NLD] = BLD(rv, lo16, (nx - NLD) * 1024, KVAUX);
      SB();
      if ((i & 1) == 0) { t0 = d; continue; }
      d = sw16(t0, d);
      if ((i & 2) == 0) { t1 = d; continue; }
      d = trs<XOR8, 8>(t1, d, lane);
      if ((i & 4) == 0) { t2 = d; continue; }
      d = trs<XOR7, 4>(t2, d, lane);
      if ((i & 8) == 0) { t3 = d; continue; }
      d = trs<XOR2, 2>(t3, d, lane);
      if ((i & 16) == 0) { t4 = d; continue; }
      d = trs<XOR1, 1>(t4, d, lane);
      sc[bt] = rbf(d) * 0.0625f;
    }
    SB();
  }
  const float mw = wave_max(fmaxf(sc[0], sc[1]));
  {
    const float p0 = __expf(sc[0] - mw), p1 = __expf(sc[1] - mw);
    const float lw = wave_sum(p0 + p1);
    const int li = ((lane >> 4) & 1) | ((lane >> 2) & 2) | (lane & 4) | ((lane << 2) & 8) | ((lane << 4) & 16);
    lds[L_P + w * 128 + half * 64 + li] = p0;
    lds[L_P + w * 128 + half * 64 + 32 + li] = p1;
    if (lane == 0) {
      u32x4 gr = {__float_as_uint(mw), tlo, __float_as_uint(lw), tlo};
      __builtin_amdgcn_raw_buffer_store_b128(gr, rs, WS_ML + ((b * H_ + h) * 64 + s * 8 + w) * 16, 0, 16);
    }
  }
  SB();

  u32x4 Wo[NWO];
  const int wvo = l56 * 16;
  constexpr int WO0 = (WV + NWO) - WK;
#pragma unroll
  for (int i = 0; i < WO0; ++i) Wo[i] = BLD(ro, wvo, i * 896, 0);
  SB();
  float acc[8];
#pragma unroll
  for (int k = 0; k < 8; ++k) acc[k] = 0.f;
  u32x4 mlg, zg;
  const int ml_off = WS_ML + ((b * H_ + h) * 64 + lane) * 16;
  const int z_off = WS_Z + ((b * 56 + ci) * 128) * 64 + t * 16;
  f32x4 p4;
#pragma unroll
  for (int j = 0; j < NLD; ++j) {
    if ((j & 3) == 0) p4 = *reinterpret_cast<const f32x4*>(lds + L_P + w * 128 + half * 64 + j);
    const float p = p4[j & 3];
#pragma unroll
    for (int k = 0; k < 4; ++k) {
      acc[2 * k] = fmaf(p, bflo(V[j][k]), acc[2 * k]);
      acc[2 * k + 1] = fmaf(p, bfhi(V[j][k]), acc[2 * k + 1]);
    }
    PIN8(acc);
    SB();
    if (j < NWO - WO0) Wo[WO0 + j] = BLD(ro, wvo, (WO0 + j) * 896, 0);
    else if (WK + (j - (NWO - WO0)) < NLD) V[WK + (j - (NWO - WO0))] = BLD(rv, lo16, (WK + (j - (NWO - WO0))) * 1024, KVAUX);
    if (j == NLD - WV + 2) {
      mlg = __builtin_amdgcn_raw_buffer_load_b128(rs, ml_off, 0, 16);
      zg = __builtin_amdgcn_raw_buffer_load_b128(rs, z_off, 0, 16);
    }
    if (j >= NLD - 16) {
      const int i = j - (NLD - 16);
#pragma unroll
      for (int k = 0; k < 4; ++k) {
        const unsigned a = Wo[2 * i][k], c = Wo[2 * i + 1][k];
        Wo[2 * i][k] = perm_lo(a, c); Wo[2 * i + 1][k] = perm_hi(a, c);
      }
      PIN1(Wo[2 * i]); PIN1(Wo[2 * i + 1]);
    }
    SB();
  }
  {
    unsigned spins = 0;
    while (!__all(mlg[1] == tlo && mlg[3] == tlo)) {
      __builtin_amdgcn_s_sleep(1);
      asm volatile("" ::: "memory");
      mlg = __builtin_amdgcn_raw_buffer_load_b128(rs, ml_off, 0, 16);
      if (++spins > (1u << 18)) break;
    }
  }
  {
    const float mi = __uint_as_float(mlg[0]), li = __uint_as_float(mlg[2]);
    const float M = wave_max(mi);
    const float L = wave_sum(li * __expf(mi - M));
    const float f = __expf(mw - M) / L;
    f32x4 oh;
#pragma unroll
    for (int k = 0; k < 4; ++k) oh[k] = sw32(acc[k], acc[k + 4]) * f;
    *reinterpret_cast<f32x4*>(lds + L_QRED + w * 256 + l32 * 8 + half * 4) = oh;
  }
  bar();
  {
    float ov = 0.f;
#pragma unroll
    for (int ww = 0; ww < 8; ++ww) ov += lds[L_QRED + ww * 256 + w * 32 + l32];
    const float on = dpp<XOR1>(ov);
    const unsigned opk = pk_bf16(ov, on);
    float ya[8];
#pragma unroll
    for (int k = 0; k < 8; ++k) ya[k] = 0.f;
#pragma unroll
    for (int i = 0; i < NWO / 2; ++i) {
      const unsigned so = __builtin_amdgcn_readlane(opk, 2 * i);
#pragma unroll
      for (int k = 0; k < 4; ++k) {
        ya[2 * k] = dot2(Wo[2 * i][k], so, ya[2 * k]);
        ya[2 * k + 1] = dot2(Wo[2 * i + 1][k], so, ya[2 * k + 1]);
      }
    }
    if (lane < 56) {
      *reinterpret_cast<f32x4*>(lds + L_YRED + w * 448 + lane * 8) = f32x4{ya[0], ya[1], ya[2], ya[3]};
      *reinterpret_cast<f32x4*>(lds + L_YRED + w * 448 + lane * 8 + 4) = f32x4{ya[4], ya[5], ya[6], ya[7]};
    }
  }
  bar();
  if (t < 224) {
    f32x2 yy = {0.f, 0.f};
#pragma unroll
    for (int ww = 0; ww < 8; ++ww) yy += *reinterpret_cast<const f32x2*>(lds + L_YRED + ww * 448 + 2 * t);
    const unsigned d0 = (__float_as_uint(yy[0]) + 0x80u) >> 8, d1 = (__float_as_uint(yy[1]) + 0x80u) >> 8;
    u32x4 gr = {d0 | g0tag, g1tag, d1 | g0tag, g1tag};
    __builtin_amdgcn_raw_buffer_store_b128(gr, rs, WS_Y + ((b * 56 + (t >> 2)) * 128 + pidx) * 64 + (t & 3) * 16, 0, 16);
  }
  if (!is_cons) return;

  {
    const int y_off = WS_Y + ((b * 56 + ci) * 128) * 64 + t * 16;
    u32x4 yg = __builtin_amdgcn_raw_buffer_load_b128(rs, y_off, 0, 16);
    const bool zlive = t < 448;
    unsigned spins = 0;
    while (!__all(!zlive || ((zg[0] & 0xff000000u) == g0tag && zg[1] == g1tag && (zg[2] & 0xff000000u) == g0tag && zg[3] == g1tag))) {
      __builtin_amdgcn_s_sleep(1);
      asm volatile("" ::: "memory");
      zg = __builtin_amdgcn_raw_buffer_load_b128(rs, z_off, 0, 16);
      if (++spins > (1u << 18)) break;
    }
    float z0 = zlive ? __uint_as_float(zg[0] << 8) : 0.f, z1 = zlive ? __uint_as_float(zg[2] << 8) : 0.f;
    z0 += dpp<ROR4>(z0); z0 += dpp<XOR8>(z0); z0 = x16_sum(z0); z0 = x32_sum(z0);
    z1 += dpp<ROR4>(z1); z1 += dpp<XOR8>(z1); z1 = x16_sum(z1); z1 = x32_sum(z1);
    spins = 0;
    while (!__all((yg[0] & 0xff000000u) == g0tag && yg[1] == g1tag && (yg[2] & 0xff000000u) == g0tag && yg[3] == g1tag)) {
      __builtin_amdgcn_s_sleep(1);
      asm volatile("" ::: "memory");
      yg = __builtin_amdgcn_raw_buffer_load_b128(rs, y_off, 0, 16);
      if (++spins > (1u << 18)) break;
    }
    float y0 = __uint_as_float(yg[0] << 8), y1 = __uint_as_float(yg[2] << 8);
    y0 += dpp<ROR4>(y0); y0 += dpp<XOR8>(y0); y0 = x16_sum(y0); y0 = x32_sum(y0);
    y1 += dpp<ROR4>(y1); y1 += dpp<XOR8>(y1); y1 = x16_sum(y1); y1 = x32_sum(y1);
    if (lane < 4) {
      *reinterpret_cast<f32x2*>(lds + L_C + w * 16 + lane * 2) = f32x2{y0, y1};
      *reinterpret_cast<f32x2*>(lds + L_C + w * 16 + 8 + lane * 2) = f32x2{z0, z1};
    }
    bar();
    if (t < 8) {
      float a = 0.f, f = 0.f;
#pragma unroll
      for (int ww = 0; ww < 8; ++ww) { a += lds[L_C + ww * 16 + t]; f += lds[L_C + ww * 16 + 8 + t]; }
      const unsigned xo = reinterpret_cast<const u16*>(ldsu + L_X)[ci * 8 + t];
      const float xr = __uint_as_float(xo << 16);
      const float res = rbf(xr + rbf(a)) + rbf(f);
      out[b * D_ + ci * 8 + t] = __builtin_bit_cast(u16, (__bf16)res);
    }
    if (ci == 0 && t == 0) {
      u32x2 ne = {tlo, thi_full};
      __builtin_amdgcn_raw_buffer_store_b64(ne, rs, WS_EPOCH + b * 256, 0, 16);
    }
  }
}

extern "C" void kernel_launch(void* const* d_in, const int* in_sizes, int n_in,
                              void* d_out, int out_size, void* d_ws, size_t ws_size,
                              hipStream_t stream) {
  const u16* x = (const u16*)d_in[0];
  const u16* k = (const u16*)d_in[1];
  const u16* v = (const u16*)d_in[2];
  const u16* wq = (const u16*)d_in[3];
  const u16* wo = (const u16*)d_in[4];
  const u16* wg = (const u16*)d_in[6];
  const u16* wu = (const u16*)d_in[7];
  const u16* wd = (const u16*)d_in[8];
  fused_kernel<<<NWG, NTHR, 0, stream>>>(x, k, v, wq, wo, wg, wu, wd, (u16*)d_out, (unsigned char*)d_ws);
}
```

```cpp
#include <hip/hip_runtime.h>
#include <hip/hip_bf16.h>
#include <stdint.h>
#include <utility>

typedef unsigned short u16;
typedef unsigned u32x4 __attribute__((ext_vector_type(4)));
typedef unsigned u32x2 __attribute__((ext_vector_type(2)));
typedef float f32x4 __attribute__((ext_vector_type(4)));
typedef float f32x2 __attribute__((ext_vector_type(2)));
typedef __bf16 bf16x2 __attribute__((ext_vector_type(2)));

constexpr int D_ = 448, H_ = 16, SK = 8192, DH = 256, F_ = 1792;
constexpr int NWG = 256, NTHR = 512;
constexpr int NSPLIT = 8;
constexpr int KEYS_WG = SK / NSPLIT;
#define WKK 48
constexpr int WK = WKK;
#define KAA 16
constexpr int KA = KAA;
#define VAA 48
constexpr int VA = VAA;
#define BARN 8
#define CODEPF 23552
#define WOP 3
#define WOS 0
constexpr int NISS = 160;
constexpr int nwo_before(int c) { return c <= WOS ? 0 : ((c - WOS) / WOP < 32 ? (c - WOS) / WOP : 32); }
constexpr bool is_wo_step(int c) { return c >= WOS && ((c - WOS) % WOP) == WOP - 1 && (c - WOS) / WOP < 32; }
constexpr int kv_of_step(int c) { return WK + c - nwo_before(c); }
constexpr int idx_of_kv(int m) {
  if (m < WK) return m;
  for (int c = 0; c < NISS; ++c) if (!is_wo_step(c) && kv_of_step(c) == m) return WK + c;
  return -1;
}
constexpr int idx_of_wo(int k) { return WK + WOS + k * WOP + WOP - 1; }
constexpr int idx_of_step(int c) { return idx_of_kv(c); }
constexpr int slot_of(int n) { return n < WK ? n : slot_of(idx_of_step(n - WK)); }
constexpr int wait_of_step(int c) { return ((WK + c < NISS) ? WK + c : NISS) - idx_of_step(c) - 1; }
static_assert(idx_of_kv(127) < NISS && idx_of_kv(127) > 0 && idx_of_wo(31) < NISS, "plan");
static_assert(idx_of_kv(112) > idx_of_wo(1) && idx_of_kv(127) > idx_of_wo(31) && idx_of_kv(120) > idx_of_wo(17), "wo landed before its pre-permute");

constexpr int WS_EPOCH = 0;
constexpr int WS_ML = 4096;
constexpr int WS_Q = WS_ML + 2 * 16 * 64 * 16;
constexpr int WS_Y = WS_Q + 2 * 16 * 1024;
constexpr int WS_Z = WS_Y + 2 * 56 * 128 * 64;
constexpr int WS_END = WS_Z + 2 * 56 * 128 * 64;

constexpr int L_XN = 0;
constexpr int L_ORED = L_XN + 8 * 256;
constexpr int L_FRED = L_ORED + 8 * 256;
constexpr int L_YRED = L_FRED + 8 * 64;
constexpr int L_P = L_YRED + 8 * 448;
constexpr int L_C = L_P + 8 * 128;
constexpr int L_X = L_C + 8 * 16;
constexpr int L_Q = L_X + 256;
constexpr int L_CNT = L_Q + 128;
constexpr int L_END = L_CNT + 4;

__device__ __forceinline__ float dot2(unsigned a, unsigned b, float c) {
  return __builtin_amdgcn_fdot2_f32_bf16(__builtin_bit_cast(bf16x2, a), __builtin_bit_cast(bf16x2, b), c, false);
}
__device__ __forceinline__ float bflo(unsigned v) { return __uint_as_float(v << 16); }
__device__ __forceinline__ float bfhi(unsigned v) { return __uint_as_float(v & 0xffff0000u); }
__device__ __forceinline__ unsigned pk_bf16(float lo, float hi) {
  bf16x2 r; r[0] = (__bf16)lo; r[1] = (__bf16)hi; return __builtin_bit_cast(unsigned, r);
}
__device__ __forceinline__ float rbf(float x) { return (float)(__bf16)x; }
__device__ __forceinline__ unsigned perm_lo(unsigned a, unsigned b) { return __builtin_amdgcn_perm(b, a, 0x05040100u); }
__device__ __forceinline__ unsigned perm_hi(unsigned a, unsigned b) { return __builtin_amdgcn_perm(b, a, 0x07060302u); }
template <int CTRL> __device__ __forceinline__ float dpp(float x) {
  return __builtin_bit_cast(float, __builtin_amdgcn_mov_dpp(__builtin_bit_cast(int, x), CTRL, 0xf, 0xf, true));
}
constexpr int XOR1 = 0xB1, XOR2 = 0x4E, XOR7 = 0x141, XOR8 = 0x128, ROR4 = 0x124;
__device__ __forceinline__ float sw16(float a, float b) {
  auto s = __builtin_amdgcn_permlane16_swap(__float_as_uint(a), __float_as_uint(b), false, false);
  return __uint_as_float(s[0]) + __uint_as_float(s[1]);
}
__device__ __forceinline__ float sw32(float a, float b) {
  auto s = __builtin_amdgcn_permlane32_swap(__float_as_uint(a), __float_as_uint(b), false, false);
  return __uint_as_float(s[0]) + __uint_as_float(s[1]);
}
__device__ __forceinline__ float x16_sum(float x) { return sw16(x, x); }
__device__ __forceinline__ float x32_sum(float x) { return sw32(x, x); }
__device__ __forceinline__ float x16_max(float x) {
  auto s = __builtin_amdgcn_permlane16_swap(__float_as_uint(x), __float_as_uint(x), false, false);
  return fmaxf(__uint_as_float(s[0]), __uint_as_float(s[1]));
}
__device__ __forceinline__ float x32_max(float x) {
  auto s = __builtin_amdgcn_permlane32_swap(__float_as_uint(x), __float_as_uint(x), false, false);
  return fmaxf(__uint_as_float(s[0]), __uint_as_float(s[1]));
}
__device__ __forceinline__ float wave_sum(float x) {
  x += dpp<XOR1>(x); x += dpp<XOR2>(x); x += dpp<XOR7>(x); x += dpp<XOR8>(x);
  x = x16_sum(x); x = x32_sum(x); return x;
}
__device__ __forceinline__ float wave_max(float x) {
  x = fmaxf(x, dpp<XOR1>(x)); x = fmaxf(x, dpp<XOR2>(x)); x = fmaxf(x, dpp<XOR7>(x)); x = fmaxf(x, dpp<XOR8>(x));
  x = x16_max(x); x = x32_max(x); return x;
}
template <int CTRL, int BIT> __device__ __forceinline__ float trs(float lo, float hi, int lane) {
  const float t1 = lo + dpp<CTRL>(lo), t2 = hi + dpp<CTRL>(hi);
  return (lane & BIT) ? t2 : t1;
}
__device__ __forceinline__ void bar() { asm volatile("s_waitcnt lgkmcnt(0)\n\ts_barrier" ::: "memory"); }
#define SB() __builtin_amdgcn_sched_barrier(0)
#define PIN8(a_) asm volatile("" : "+v"(a_[0]), "+v"(a_[1]), "+v"(a_[2]), "+v"(a_[3]), "+v"(a_[4]), "+v"(a_[5]), "+v"(a_[6]), "+v"(a_[7]))
#define PIN1(a_) asm volatile("" : "+v"(a_))
#define MKRS(ptr_, bytes_) __builtin_amdgcn_make_buffer_rsrc((void*)(ptr_), 0, (bytes_), 0x00020000)
#define STAMP(i_)

__device__ __forceinline__ u32x4 mkdesc(const void* p, unsigned bytes) {
  const unsigned long long a = (unsigned long long)p;
  u32x4 d;
  d[0] = __builtin_amdgcn_readfirstlane((unsigned)a);
  d[1] = __builtin_amdgcn_readfirstlane((unsigned)(a >> 32) & 0xffffu);
  d[2] = __builtin_amdgcn_readfirstlane(bytes);
  d[3] = 0x00020000u;
  return d;
}
template <int OFF> __device__ __forceinline__ void ld_nt_new(u32x4& d, int voff, const u32x4& rsrc, int soff) {
  asm volatile("buffer_load_dwordx4 %0, %1, %2, %3 offen offset:%4 nt" : "=v"(d) : "v"(voff), "s"(rsrc), "s"(soff), "i"(OFF));
}
template <int OFF> __device__ __forceinline__ void ld_nt_re(u32x4& d, int voff, const u32x4& rsrc, int soff) {
  asm volatile("buffer_load_dwordx4 %0, %1, %2, %3 offen offset:%4 nt" : "+v"(d) : "v"(voff), "s"(rsrc), "s"(soff), "i"(OFF));
}
#define WO_POL " nt"
#define W_POL " nt"
#define C_POL ""
template <int OFF> __device__ __forceinline__ void ld_pl_re(u32x4& d, int voff, const u32x4& rsrc, int soff) {
  asm volatile("buffer_load_dwordx4 %0, %1, %2, %3 offen offset:%4" WO_POL : "+v"(d) : "v"(voff), "s"(rsrc), "s"(soff), "i"(OFF));
}
__device__ __forceinline__ void ld_g(u32x4& d, const void* p) {
  asm volatile("global_load_dwordx4 %0, %1, off" W_POL : "=v"(d) : "v"(p));
}
__device__ __forceinline__ void ld_b(u32x4& d, int voff, const u32x4& rsrc, int soff) {
  asm volatile("buffer_load_dwordx4 %0, %1, %2, %3 offen" C_POL : "=v"(d) : "v"(voff), "s"(rsrc), "s"(soff));
}
__device__ __forceinline__ void ld_sc1(u32x4& d, int voff, const u32x4& rsrc) {
  asm volatile("buffer_load_dwordx4 %0, %1, %2, 0 offen sc1" : "=v"(d) : "v"(voff), "s"(rsrc) : "memory");
}
template <int N> __device__ __forceinline__ void waitv(u32x4& d) { asm volatile("s_waitcnt vmcnt(%1)" : "+v"(d) : "i"(N)); }

struct Ctx {
  u32x4 rk, rv, ro;
  int lo16, wvo;
};
template <int n> __device__ __forceinline__ void issue(u32x4 (&R)[WK], const Ctx& c) {
  if constexpr (n < NISS) {
    constexpr int sl = slot_of(n);
    if constexpr (n < WK) {
      if constexpr (n >= KA) ld_nt_new<(n & 3) * 1024>(R[sl], c.lo16, c.rk, (n >> 2) * 32768);
      else ld_nt_re<(n & 3) * 1024>(R[sl], c.lo16, c.rk, (n >> 2) * 32768);
    } else if constexpr (is_wo_step(n - WK)) {
      constexpr int k = nwo_before(n - WK);
      ld_pl_re<(k & 3) * 896>(R[sl], c.wvo, c.ro, (k >> 2) * 3584);
    } else {
      constexpr int m = kv_of_step(n - WK);
      if constexpr (m < 64) ld_nt_re<(m & 3) * 1024>(R[sl], c.lo16, c.rk, (m >> 2) * 32768);
      else ld_nt_re<((m - 64) & 3) * 1024>(R[sl], c.lo16, c.rv, ((m - 64) >> 2) * 32768);
    }
  }
}
template <int A, int... I> __device__ __forceinline__ void issue_range(u32x4 (&R)[WK], const Ctx& c, std::integer_sequence<int, I...>) {
  (issue<A + I>(R, c), ...);
}

struct KState { float t0, t1, t2, t3, t4; float sc[2]; };
template <int c> __device__ __forceinline__ void kstep(u32x4 (&R)[WK], const Ctx& cx, const u32x4& q4, KState& st, int lane) {
  constexpr int sl = slot_of(idx_of_step(c));
  waitv<wait_of_step(c)>(R[sl]);
  float d = dot2(R[sl][0], q4[0], 0.f);
  d = dot2(R[sl][1], q4[1], d);
  d = dot2(R[sl][2], q4[2], d);
  d = dot2(R[sl][3], q4[3], d);
  PIN1(d);
  issue<WK + c>(R, cx);
  if constexpr (BARN && (c % BARN) == BARN - 1) __builtin_amdgcn_s_barrier();
  constexpr int i = c & 31;
  if constexpr ((i & 1) == 0) { st.t0 = d; return; }
  else {
    d = sw16(st.t0, d);
    if constexpr ((i & 2) == 0) { st.t1 = d; return; }
    else {
      d = trs<XOR8, 8>(st.t1, d, lane);
      if constexpr ((i & 4) == 0) { st.t2 = d; return; }
      else {
        d = trs<XOR7, 4>(st.t2, d, lane);
        if constexpr ((i & 8) == 0) { st.t3 = d; return; }
        else {
          d = trs<XOR2, 2>(st.t3, d, lane);
          if constexpr ((i & 16) == 0) { st.t4 = d; return; }
          else {
            d = trs<XOR1, 1>(st.t4, d, lane);
            st.sc[c >> 5] = rbf(d) * 0.0625f;
          }
        }
      }
    }
  }
}
template <int... I> __device__ __forceinline__ void kphase(u32x4 (&R)[WK], const Ctx& cx, const u32x4& q4, KState& st, int lane, std::integer_sequence<int, I...>) {
  (kstep<I>(R, cx, q4, st, lane), ...);
}

struct VState { float acc[8]; f32x4 p4; u32x4 mlg, zg; };
constexpr int GATHER_STEP = 40;
template <int j> __device__ __forceinline__ void vstep(u32x4 (&R)[WK], const Ctx& cx, VState& st, const float* pl, const u32x4& rws, int ml_off, int z_off) {
  constexpr int c = 64 + j;
  constexpr int sl = slot_of(idx_of_step(c));
  if constexpr ((j & 3) == 0) st.p4 = *reinterpret_cast<const f32x4*>(pl + j);
  waitv<wait_of_step(c)>(R[sl]);
  const float p = st.p4[j & 3];
#pragma unroll
  for (int k = 0; k < 4; ++k) {
    st.acc[2 * k] = fmaf(p, bflo(R[sl][k]), st.acc[2 * k]);
    st.acc[2 * k + 1] = fmaf(p, bfhi(R[sl][k]), st.acc[2 * k + 1]);
  }
  PIN8(st.acc);
  issue<WK + c>(R, cx);
  if constexpr (j == GATHER_STEP) { ld_sc1(st.mlg, ml_off, rws); ld_sc1(st.zg, z_off, rws); }
  if constexpr (j >= 48) {
    constexpr int i = j - 48;
    static_assert(idx_of_kv(64 + j) > idx_of_wo(2 * i + 1), "wo pair landed");
    constexpr int sa = slot_of(idx_of_wo(2 * i)), sb = slot_of(idx_of_wo(2 * i + 1));
    PIN1(R[sa]); PIN1(R[sb]);
#pragma unroll
    for (int k = 0; k < 4; ++k) {
      const unsigned a = R[sa][k], e = R[sb][k];
      R[sa][k] = perm_lo(a, e); R[sb][k] = perm_hi(a, e);
    }
    PIN1(R[sa]); PIN1(R[sb]);
  }
  if constexpr (BARN && (j % BARN) == BARN - 1 && j < 48) __builtin_amdgcn_s_barrier();
}
template <int... I> __device__ __forceinline__ void vphase(u32x4 (&R)[WK], const Ctx& cx, VState& st, const float* pl, const u32x4& rws, int ml_off, int z_off, std::integer_sequence<int, I...>) {
  (vstep<I>(R, cx, st, pl, rws, ml_off, z_off), ...);
}
template <int i> __device__ __forceinline__ void ostep(u32x4 (&R)[WK], unsigned opk, float (&ya)[8]) {
  constexpr int sa = slot_of(idx_of_wo(2 * i)), sb = slot_of(idx_of_wo(2 * i + 1));
  const unsigned so = __builtin_amdgcn_readlane(opk, 2 * i);
#pragma unroll
  for (int k = 0; k < 4; ++k) {
    ya[2 * k] = dot2(R[sa][k], so, ya[2 * k]);
    ya[2 * k + 1] = dot2(R[sb][k], so, ya[2 * k + 1]);
  }
}
template <int... I> __device__ __forceinline__ void ophase(u32x4 (&R)[WK], unsigned opk, float (&ya)[8], std::integer_sequence<int, I...>) {
  (ostep<I>(R, opk, ya), ...);
}
static_assert(WK > 32 && KA <= WK, "plan");

__device__ __forceinline__ f32x2 gemv8(const u32x4 (&F)[8], const unsigned (&xp)[4]) {
  float fa[8];
#pragma unroll
  for (int k = 0; k < 8; ++k) fa[k] = 0.f;
#pragma unroll
  for (int i = 0; i < 4; ++i)
#pragma unroll
    for (int k = 0; k < 4; ++k) {
      fa[2 * k] = dot2(perm_lo(F[2 * i][k], F[2 * i + 1][k]), xp[i], fa[2 * k]);
      fa[2 * k + 1] = dot2(perm_hi(F[2 * i][k], F[2 * i + 1][k]), xp[i], fa[2 * k + 1]);
    }
  float f4[4];
  f32x2 f2;
#pragma unroll
  for (int k = 0; k < 4; ++k) f4[k] = sw32(fa[k], fa[k + 4]);
#pragma unroll
  for (int k = 0; k < 2; ++k) { float v = sw16(f4[k], f4[k + 2]); v += dpp<ROR4>(v); v += dpp<XOR8>(v); f2[k] = v; }
  return f2;
}

__global__ void __launch_bounds__(NTHR, 2)
fused_kernel(const u16* __restrict__ x, const u16* __restrict__ kc, const u16* __restrict__ vc,
             const u16* __restrict__ wq, const u16* __restrict__ wo, const u16* __restrict__ wg,
             const u16* __restrict__ wu, const u16* __restrict__ wd, u16* __restrict__ out,
             unsigned char* ws) {
  __shared__ __attribute__((aligned(16))) float lds[L_END];
  unsigned* ldsu = reinterpret_cast<unsigned*>(lds);
  const int t = threadIdx.x, lane = t & 63;
  const int w = __builtin_amdgcn_readfirstlane(t >> 6);
  const int g = blockIdx.x;
  const int xcd = g & 7, r = g >> 3, hsel = (r >> 3) & 1, b = r >> 4, s = r & 7;
  const int h = xcd + 8 * hsel;
  const int i16 = hsel * 8 + s;
  const int pidx = h * 8 + s;
  const bool has_ffn = i16 < 14;
  const int slice = xcd * 14 + (has_ffn ? i16 : 13);
  const int zrow = has_ffn ? slice : (112 + xcd * 2 + (i16 - 14));
  const bool is_cons = i16 >= 9;
  const int ci = xcd * 7 + (is_cons ? (i16 - 9) : 0);
  const int half = lane >> 5, l32 = lane & 31;
  const int l56 = lane < 56 ? lane : lane - 8;

  const __amdgpu_buffer_rsrc_t rs = MKRS(ws, WS_END);
  const u32x4 rws = mkdesc(ws, WS_END);
  const size_t kvoff = ((size_t)(b * H_ + h) * SK + s * KEYS_WG + w * 8) * DH;
  Ctx cx;
  cx.rk = mkdesc(kc + kvoff, 524288 - w * 4096);
  cx.rv = mkdesc(vc + kvoff, 524288 - w * 4096);
  cx.ro = mkdesc(wo + (size_t)(h * 256 + w * 32) * D_, 32 * 896);
  cx.lo16 = lane * 16;
  cx.wvo = l56 * 16;
  const u32x4 rcode = mkdesc((const void*)(__builtin_amdgcn_s_getpc() & ~63ull), w == 7 ? CODEPF : 0);

  STAMP(0);
  u32x4 R[WK];
#pragma unroll
  for (int i = 0; i < KA; ++i) asm volatile("" : "=v"(R[i]));
  u32x4 ep;
  asm volatile("s_nop 4");
  ld_sc1(ep, WS_EPOCH + b * 256, rws);
  u32x4 cpf;
  ld_b(cpf, cx.lo16, rcode, r * 1024);
  u32x4 xv, Fq[8], Fg[8], Fd[4];
  const bool worker = (w >= 1 && w <= 4);
  const int tw = t - 64;
  const int mh = t & 3, j0 = (tw >> 2) & 63;
  const int j3 = (j0 < 32) ? (j0 + 192) : j0;
  if (w == 0) {
    if (lane == 0) ldsu[L_CNT] = 0u;
    issue_range<0>(R, cx, std::make_integer_sequence<int, KA>{});
  } else if (worker) {
    ld_g(xv, x + b * D_ + l56 * 8);
    const u16* qb = wq + h * 256 + s * 32 + mh * 8;
#pragma unroll
    for (int i = 0; i < 3; ++i) {
      ld_g(Fq[2 * i], qb + (size_t)(2 * (j0 + 64 * i)) * 4096);
      ld_g(Fq[2 * i + 1], qb + (size_t)(2 * (j0 + 64 * i) + 1) * 4096);
    }
    ld_g(Fq[6], qb + (size_t)(2 * j3) * 4096);
    ld_g(Fq[7], qb + (size_t)(2 * j3 + 1) * 4096);
    const u16* base = ((mh & 2) ? wu : wg) + slice * 16 + (mh & 1) * 8;
#pragma unroll
    for (int i = 0; i < 3; ++i) {
      ld_g(Fg[2 * i], base + (size_t)(2 * (j0 + 64 * i)) * F_);
      ld_g(Fg[2 * i + 1], base + (size_t)(2 * (j0 + 64 * i) + 1) * F_);
    }
    ld_g(Fg[6], base + (size_t)(2 * j3) * F_);
    ld_g(Fg[7], base + (size_t)(2 * j3 + 1) * F_);
    const u16* pd = wd + (size_t)(slice * 16 + 4 * (w - 1)) * D_ + l56 * 8;
#pragma unroll
    for (int i = 0; i < 4; ++i) ld_g(Fd[i], pd + i * D_);
  }
  bar();
  if (w == 0) {
    __builtin_amdgcn_s_setprio(3);
    waitv<KA>(ep);
    const unsigned tl0 = __builtin_amdgcn_readfirstlane(ep[0]) + 1u;
    {
      unsigned spins = 0;
      while (__hip_atomic_load(ldsu + L_CNT, __ATOMIC_RELAXED, __HIP_MEMORY_SCOPE_WORKGROUP) != 4u && ++spins < (1u << 22)) __builtin_amdgcn_s_sleep(1);
      asm volatile("" ::: "memory");
    }
    STAMP(2);
    if (lane < 8) {
      f32x4 a = {0.f, 0.f, 0.f, 0.f};
#pragma unroll
      for (int ww = 0; ww < 4; ++ww) a += *reinterpret_cast<const f32x4*>(lds + L_FRED + ww * 64 + 32 + lane * 4);
      u32x4 gr = {pk_bf16(a[0], a[1]), tl0, pk_bf16(a[2], a[3]), tl0};
      __builtin_amdgcn_raw_buffer_store_b128(gr, rs, WS_Q + (b * H_ + h) * 1024 + s * 128 + lane * 16, 0, 16);
    }
    const int q_off = WS_Q + (b * H_ + h) * 1024 + lane * 16;
    u32x4 qg;
    unsigned spins = 0;
    do {
      ld_sc1(qg, q_off, rws);
      waitv<0>(qg);
    } while (!__all(qg[1] == tl0 && qg[3] == tl0) && ++spins < (1u << 18));
    *reinterpret_cast<u32x2*>(ldsu + L_Q + lane * 2) = u32x2{qg[0], qg[2]};
    __builtin_amdgcn_s_setprio(0);
    STAMP(3);
  } else if (worker) {
    waitv<20>(xv);
    {
      float xf[8];
#pragma unroll
      for (int k = 0; k < 4; ++k) { xf[2 * k] = bflo(xv[k]); xf[2 * k + 1] = bfhi(xv[k]); }
      float sm = 0.f;
#pragma unroll
      for (int k = 0; k < 8; ++k) sm += xf[k];
      sm = lane < 56 ? sm : 0.f;
      sm = wave_sum(sm);
      const float mexact = sm / (float)D_;
      const float m = rbf(mexact);
      float vs = 0.f;
#pragma unroll
      for (int k = 0; k < 8; ++k) { float d = xf[k] - mexact; vs += d * d; }
      vs = lane < 56 ? vs : 0.f;
      vs = wave_sum(vs);
      const float var = rbf(vs / (float)D_);
      const float den = rbf(sqrtf(rbf(var + 1e-5f)));
      u32x4 xn;
#pragma unroll
      for (int k = 0; k < 4; ++k) xn[k] = pk_bf16(rbf(xf[2 * k] - m) / den, rbf(xf[2 * k + 1] - m) / den);
      if (lane < 56) {
        *reinterpret_cast<u32x4*>(ldsu + L_XN + w * 256 + lane * 4) = xn;
        if (w == 1) *reinterpret_cast<u32x4*>(ldsu + L_X + lane * 4) = xv;
      }
    }
    STAMP(1);
    unsigned xp[4];
#pragma unroll
    for (int i = 0; i < 3; ++i) xp[i] = ldsu[L_XN + w * 256 + j0 + 64 * i];
    xp[3] = ldsu[L_XN + w * 256 + j3];
    xp[3] = (j0 < 32) ? xp[3] : 0u;
    const int fo = L_FRED + (w - 1) * 64 + mh * 8 + (lane >> 5) * 4 + ((lane >> 4) & 1) * 2;
    asm volatile("s_waitcnt vmcnt(12)" : "+v"(Fq[0]), "+v"(Fq[1]), "+v"(Fq[2]), "+v"(Fq[3]), "+v"(Fq[4]), "+v"(Fq[5]), "+v"(Fq[6]), "+v"(Fq[7]));
    const f32x2 q2 = gemv8(Fq, xp);
    if ((lane & 12) == 0) *reinterpret_cast<f32x2*>(lds + fo + 32) = q2;
    asm volatile("s_waitcnt vmcnt(4)" : "+v"(Fg[0]), "+v"(Fg[1]), "+v"(Fg[2]), "+v"(Fg[3]), "+v"(Fg[4]), "+v"(Fg[5]), "+v"(Fg[6]), "+v"(Fg[7]));
    const f32x2 f2 = gemv8(Fg, xp);
    if ((lane & 12) == 0) *reinterpret_cast<f32x2*>(lds + fo) = f2;
    asm volatile("s_waitcnt lgkmcnt(0)" ::: "memory");
    if (lane == 0) __hip_atomic_fetch_add(ldsu + L_CNT, 1u, __ATOMIC_RELAXED, __HIP_MEMORY_SCOPE_WORKGROUP);
    STAMP(2);
    {
      unsigned spins = 0;
      while (__hip_atomic_load(ldsu + L_CNT, __ATOMIC_RELAXED, __HIP_MEMORY_SCOPE_WORKGROUP) != 4u && ++spins < (1u << 22)) __builtin_amdgcn_s_sleep(1);
      asm volatile("" ::: "memory");
    }
    f32x4 gg = {0.f, 0.f, 0.f, 0.f}, uu = {0.f, 0.f, 0.f, 0.f};
#pragma unroll
    for (int ww = 0; ww < 4; ++ww) {
      gg += *reinterpret_cast<const f32x4*>(lds + L_FRED + ww * 64 + 4 * (w - 1));
      uu += *reinterpret_cast<const f32x4*>(lds + L_FRED + ww * 64 + 16 + 4 * (w - 1));
    }
    float hm[4];
#pragma unroll
    for (int k = 0; k < 4; ++k) {
      const float gv = rbf(gg[k]), uv = rbf(uu[k]);
      const float sg = rbf(gv / (1.f + __expf(-gv)));
      hm[k] = sg * uv;
    }
    const unsigned hp0 = pk_bf16(hm[0], hm[1]), hp1 = pk_bf16(hm[2], hm[3]);
    asm volatile("s_waitcnt vmcnt(0)" : "+v"(Fd[0]), "+v"(Fd[1]), "+v"(Fd[2]), "+v"(Fd[3]));
    f32x4 z0, z1;
#pragma unroll
    for (int k = 0; k < 2; ++k) {
      z0[2 * k] = dot2(perm_lo(Fd[2][k], Fd[3][k]), hp1, dot2(perm_lo(Fd[0][k], Fd[1][k]), hp0, 0.f));
      z0[2 * k + 1] = dot2(perm_hi(Fd[2][k], Fd[3][k]), hp1, dot2(perm_hi(Fd[0][k], Fd[1][k]), hp0, 0.f));
      z1[2 * k] = dot2(perm_lo(Fd[2][k + 2], Fd[3][k + 2]), hp1, dot2(perm_lo(Fd[0][k + 2], Fd[1][k + 2]), hp0, 0.f));
      z1[2 * k + 1] = dot2(perm_hi(Fd[2][k + 2], Fd[3][k + 2]), hp1, dot2(perm_hi(Fd[0][k + 2], Fd[1][k + 2]), hp0, 0.f));
    }
    if (lane < 56) {
      *reinterpret_cast<f32x4*>(lds + L_YRED + (w - 1) * 448 + lane * 8) = z0;
      *reinterpret_cast<f32x4*>(lds + L_YRED + (w - 1) * 448 + lane * 8 + 4) = z1;
    }
    STAMP(3);
  }
  if (w != 0) issue_range<0>(R, cx, std::make_integer_sequence<int, KA>{});
  asm volatile("" ::: "memory");
  if (w == 0) bar();
  issue_range<KA>(R, cx, std::make_integer_sequence<int, WK - KA>{});
  if (w != 0) bar();
  waitv<WK - KA>(ep);
  PIN1(cpf);
  const unsigned long long e64 = (((unsigned long long)ep[1] << 32) | ep[0]) + 1ull;
  const unsigned tlo = __builtin_amdgcn_readfirstlane((unsigned)e64);
  const unsigned thi_full = __builtin_amdgcn_readfirstlane((unsigned)(e64 >> 32));
  const unsigned thi = thi_full & 0xffu;
  const unsigned g0tag = tlo << 24, g1tag = (tlo >> 8) | (thi << 24);
  const u32x4 q4 = *reinterpret_cast<const u32x4*>(ldsu + L_Q + l32 * 4);
  if (t < 224) {
    f32x2 zz = {0.f, 0.f};
#pragma unroll
    for (int ww = 0; ww < 4; ++ww) zz += *reinterpret_cast<const f32x2*>(lds + L_YRED + ww * 448 + 2 * t);
    const unsigned d0 = (__float_as_uint(zz[0]) + 0x80u) >> 8, d1 = (__float_as_uint(zz[1]) + 0x80u) >> 8;
    u32x4 gr = {d0 | g0tag, g1tag, d1 | g0tag, g1tag};
    __builtin_amdgcn_raw_buffer_store_b128(gr, rs, WS_Z + ((b * 56 + (t >> 2)) * 128 + zrow) * 64 + (t & 3) * 16, 0, 16);
  }
  STAMP(4);

  KState ks;
  kphase(R, cx, q4, ks, lane, std::make_integer_sequence<int, 64>{});
  STAMP(5);
  const float mw = wave_max(fmaxf(ks.sc[0], ks.sc[1]));
  {
    const float p0 = __expf(ks.sc[0] - mw), p1 = __expf(ks.sc[1] - mw);
    const float lw = wave_sum(p0 + p1);
    const int li = ((lane >> 4) & 1) | ((lane >> 2) & 2) | (lane & 4) | ((lane << 2) & 8) | ((lane << 4) & 16);
    lds[L_P + w * 128 + half * 64 + li] = p0;
    lds[L_P + w * 128 + half * 64 + 32 + li] = p1;
    if (lane == 0) {
      u32x4 gr = {__float_as_uint(mw), tlo, __float_as_uint(lw), tlo};
      __builtin_amdgcn_raw_buffer_store_b128(gr, rs, WS_ML + ((b * H_ + h) * 64 + s * 8 + w) * 16, 0, 16);
    }
  }

  VState vs;
#pragma unroll
  for (int k = 0; k < 8; ++k) vs.acc[k] = 0.f;
  const int ml_off = WS_ML + ((b * H_ + h) * 64 + lane) * 16;
  const int z_off = WS_Z + ((b * 56 + ci) * 128) * 64 + t * 16;
  vphase(R, cx, vs, lds + L_P + w * 128 + half * 64, rws, ml_off, z_off, std::make_integer_sequence<int, 64>{});
  asm volatile("s_waitcnt vmcnt(0)" : "+v"(vs.mlg), "+v"(vs.zg));
  STAMP(6);
  {
    unsigned spins = 0;
    while (!__all(vs.mlg[1] == tlo && vs.mlg[3] == tlo) && ++spins < (1u << 18)) {
      __builtin_amdgcn_s_sleep(1);
      ld_sc1(vs.mlg, ml_off, rws);
      waitv<0>(vs.mlg);
    }
  }
  {
    const float mi = __uint_as_float(vs.mlg[0]), li = __uint_as_float(vs.mlg[2]);
    const float M = wave_max(mi);
    const float L = wave_sum(li * __expf(mi - M));
    const float f = __expf(mw - M) / L;
    f32x4 oh;
#pragma unroll
    for (int k = 0; k < 4; ++k) oh[k] = sw32(vs.acc[k], vs.acc[k + 4]) * f;
    *reinterpret_cast<f32x4*>(lds + L_ORED + w * 256 + l32 * 8 + half * 4) = oh;
  }
  STAMP(7);
  bar();
  STAMP(8);
  {
    float ov = 0.f;
#pragma unroll
    for (int ww = 0; ww < 8; ++ww) ov += lds[L_ORED + ww * 256 + w * 32 + l32];
    const float on = dpp<XOR1>(ov);
    const unsigned opk = pk_bf16(ov, on);
    float ya[8];
#pragma unroll
    for (int k = 0; k < 8; ++k) ya[k] = 0.f;
    ophase(R, opk, ya, std::make_integer_sequence<int, 16>{});
    if (lane < 56) {
      *reinterpret_cast<f32x4*>(lds + L_YRED + w * 448 + lane * 8) = f32x4{ya[0], ya[1], ya[2], ya[3]};
      *reinterpret_cast<f32x4*>(lds + L_YRED + w * 448 + lane * 8 + 4) = f32x4{ya[4], ya[5], ya[6], ya[7]};
    }
  }
  bar();
  if (t < 224) {
    f32x2 yy = {0.f, 0.f};
#pragma unroll
    for (int ww = 0; ww < 8; ++ww) yy += *reinterpret_cast<const f32x2*>(lds + L_YRED + ww * 448 + 2 * t);
    const unsigned d0 = (__float_as_uint(yy[0]) + 0x80u) >> 8, d1 = (__float_as_uint(yy[1]) + 0x80u) >> 8;
    u32x4 gr = {d0 | g0tag, g1tag, d1 | g0tag, g1tag};
    __builtin_amdgcn_raw_buffer_store_b128(gr, rs, WS_Y + ((b * 56 + (t >> 2)) * 128 + pidx) * 64 + (t & 3) * 16, 0, 16);
  }
  STAMP(9);
  if (!is_cons) return;

  {
    const int y_off = WS_Y + ((b * 56 + ci) * 128) * 64 + t * 16;
    u32x4 yg;
    ld_sc1(yg, y_off, rws);
    const bool zlive = t < 448;
    u32x4 zg = vs.zg;
    unsigned spins = 0;
    while (!__all(!zlive || ((zg[0] & 0xff000000u) == g0tag && zg[1] == g1tag && (zg[2] & 0xff000000u) == g0tag && zg[3] == g1tag)) && ++spins < (1u << 18)) {
      __builtin_amdgcn_s_sleep(1);
      ld_sc1(zg, z_off, rws);
      waitv<0>(zg);
    }
    float z0 = zlive ? __uint_as_float(zg[0] << 8) : 0.f, z1 = zlive ? __uint_as_float(zg[2] << 8) : 0.f;
    z0 += dpp<ROR4>(z0); z0 += dpp<XOR8>(z0); z0 = x16_sum(z0); z0 = x32_sum(z0);
    z1 += dpp<ROR4>(z1); z1 += dpp<XOR8>(z1); z1 = x16_sum(z1); z1 = x32_sum(z1);
    PIN1(z0); PIN1(z1);
    waitv<0>(yg);
    spins = 0;
    while (!__all((yg[0] & 0xff000000u) == g0tag && yg[1] == g1tag && (yg[2] & 0xff000000u) == g0tag && yg[3] == g1tag) && ++spins < (1u << 18)) {
      ld_sc1(yg, y_off, rws);
      waitv<0>(yg);
    }
    float y0 = __uint_as_float(yg[0] << 8), y1 = __uint_as_float(yg[2] << 8);
    y0 += dpp<ROR4>(y0); y0 += dpp<XOR8>(y0); y0 = x16_sum(y0); y0 = x32_sum(y0);
    y1 += dpp<ROR4>(y1); y1 += dpp<XOR8>(y1); y1 = x16_sum(y1); y1 = x32_sum(y1);
    if (lane < 4) {
      *reinterpret_cast<f32x2*>(lds + L_C + w * 16 + lane * 2) = f32x2{y0, y1};
      *reinterpret_cast<f32x2*>(lds + L_C + w * 16 + 8 + lane * 2) = f32x2{z0, z1};
    }
    STAMP(10);
    bar();
    if (t < 8) {
      float a = 0.f, f = 0.f;
#pragma unroll
      for (int ww = 0; ww < 8; ++ww) { a += lds[L_C + ww * 16 + t]; f += lds[L_C + ww * 16 + 8 + t]; }
      const unsigned xo = reinterpret_cast<const u16*>(ldsu + L_X)[ci * 8 + t];
      const float xr = __uint_as_float(xo << 16);
      const float res = rbf(xr + rbf(a)) + rbf(f);
      out[b * D_ + ci * 8 + t] = __builtin_bit_cast(u16, (__bf16)res);
    }
    STAMP(11);
    if (ci == 0 && t == 0) {
      u32x2 ne = {tlo, thi_full};
      __builtin_amdgcn_raw_buffer_store_b64(ne, rs, WS_EPOCH + b * 256, 0, 16);
    }
  }
}

extern "C" void kernel_launch(void* const* d_in, const int* in_sizes, int n_in,
                              void* d_out, int out_size, void* d_ws, size_t ws_size,
                              hipStream_t stream) {
  const u16* x = (const u16*)d_in[0];
  const u16* k = (const u16*)d_in[1];
  const u16* v = (const u16*)d_in[2];
  const u16* wq = (const u16*)d_in[3];
  const u16* wo = (const u16*)d_in[4];
  const u16* wg = (const u16*)d_in[6];
  const u16* wu = (const u16*)d_in[7];
  const u16* wd = (const u16*)d_in[8];
  fused_kernel<<<NWG, NTHR, 0, stream>>>(x, k, v, wq, wo, wg, wu, wd, (u16*)d_out, (unsigned char*)d_ws);
}
```

```cpp
#include <hip/hip_runtime.h>
#include <hip/hip_bf16.h>
#include <stdint.h>
#include <utility>

typedef unsigned short u16;
typedef unsigned u32x4 __attribute__((ext_vector_type(4)));
typedef unsigned u32x2 __attribute__((ext_vector_type(2)));
typedef float f32x4 __attribute__((ext_vector_type(4)));
typedef float f32x2 __attribute__((ext_vector_type(2)));
typedef __bf16 bf16x2 __attribute__((ext_vector_type(2)));

constexpr int D_ = 448, H_ = 16, SK = 8192, DH = 256, F_ = 1792;
constexpr int NWG = 256, NTHR = 512;
constexpr int NSPLIT = 8;
constexpr int KEYS_WG = SK / NSPLIT;
#define WKK 48
constexpr int WK = WKK;
#define KAA 16
constexpr int KA = KAA;
#define VAA 48
constexpr int VA = VAA;
#define BARN 8
#define CODEPF 23552
#define WOP 3
#define WOS 0
constexpr int NISS = 160;
constexpr int nwo_before(int c) { return c <= WOS ? 0 : ((c - WOS) / WOP < 32 ? (c - WOS) / WOP : 32); }
constexpr bool is_wo_step(int c) { return c >= WOS && ((c - WOS) % WOP) == WOP - 1 && (c - WOS) / WOP < 32; }
constexpr int kv_of_step(int c) { return WK + c - nwo_before(c); }
constexpr int idx_of_kv(int m) {
  if (m < WK) return m;
  for (int c = 0; c < NISS; ++c) if (!is_wo_step(c) && kv_of_step(c) == m) return WK + c;
  return -1;
}
constexpr int idx_of_wo(int k) { return WK + WOS + k * WOP + WOP - 1; }
constexpr int idx_of_step(int c) { return idx_of_kv(c); }
constexpr int slot_of(int n) { return n < WK ? n : slot_of(idx_of_step(n - WK)); }
constexpr int wait_of_step(int c) { return ((WK + c < NISS) ? WK + c : NISS) - idx_of_step(c) - 1; }
static_assert(idx_of_kv(127) < NISS && idx_of_kv(127) > 0 && idx_of_wo(31) < NISS, "plan");
static_assert(idx_of_kv(112) > idx_of_wo(1) && idx_of_kv(127) > idx_of_wo(31) && idx_of_kv(120) > idx_of_wo(17), "wo landed before its pre-permute");

constexpr int WS_EPOCH = 0;
constexpr int WS_ML = 4096;
constexpr int WS_Q = WS_ML + 2 * 16 * 64 * 16;
constexpr int WS_Y = WS_Q + 2 * 16 * 1024;
constexpr int WS_Z = WS_Y + 2 * 56 * 128 * 64;
constexpr int WS_END = WS_Z + 2 * 56 * 128 * 64;

constexpr int L_XN = 0;
constexpr int L_ORED = L_XN + 8 * 256;
constexpr int L_FRED = L_ORED + 8 * 256;
constexpr int L_YRED = L_FRED + 8 * 64;
constexpr int L_P = L_YRED + 8 * 448;
constexpr int L_C = L_P + 8 * 128;
constexpr int L_X = L_C + 8 * 16;
constexpr int L_Q = L_X + 256;
constexpr int L_CNT = L_Q + 128;
constexpr int L_END = L_CNT + 4;

__device__ __forceinline__ float dot2(unsigned a, unsigned b, float c) {
  return __builtin_amdgcn_fdot2_f32_bf16(__builtin_bit_cast(bf16x2, a), __builtin_bit_cast(bf16x2, b), c, false);
}
__device__ __forceinline__ float bflo(unsigned v) { return __uint_as_float(v << 16); }
__device__ __forceinline__ float bfhi(unsigned v) { return __uint_as_float(v & 0xffff0000u); }
__device__ __forceinline__ unsigned pk_bf16(float lo, float hi) {
  bf16x2 r; r[0] = (__bf16)lo; r[1] = (__bf16)hi; return __builtin_bit_cast(unsigned, r);
}
__device__ __forceinline__ float rbf(float x) { return (float)(__bf16)x; }
__device__ __forceinline__ unsigned perm_lo(unsigned a, unsigned b) { return __builtin_amdgcn_perm(b, a, 0x05040100u); }
__device__ __forceinline__ unsigned perm_hi(unsigned a, unsigned b) { return __builtin_amdgcn_perm(b, a, 0x07060302u); }
template <int CTRL> __device__ __forceinline__ float dpp(float x) {
  return __builtin_bit_cast(float, __builtin_amdgcn_mov_dpp(__builtin_bit_cast(int, x), CTRL, 0xf, 0xf, true));
}
constexpr int XOR1 = 0xB1, XOR2 = 0x4E, XOR7 = 0x141, XOR8 = 0x128, ROR4 = 0x124;
__device__ __forceinline__ float sw16(float a, float b) {
  auto s = __builtin_amdgcn_permlane16_swap(__float_as_uint(a), __float_as_uint(b), false, false);
  return __uint_as_float(s[0]) + __uint_as_float(s[1]);
}
__device__ __forceinline__ float sw32(float a, float b) {
  auto s = __builtin_amdgcn_permlane32_swap(__float_as_uint(a), __float_as_uint(b), false, false);
  return __uint_as_float(s[0]) + __uint_as_float(s[1]);
}
__device__ __forceinline__ float x16_sum(float x) { return sw16(x, x); }
__device__ __forceinline__ float x32_sum(float x) { return sw32(x, x); }
__device__ __forceinline__ float x16_max(float x) {
  auto s = __builtin_amdgcn_permlane16_swap(__float_as_uint(x), __float_as_uint(x), false, false);
  return fmaxf(__uint_as_float(s[0]), __uint_as_float(s[1]));
}
__device__ __forceinline__ float x32_max(float x) {
  auto s = __builtin_amdgcn_permlane32_swap(__float_as_uint(x), __float_as_uint(x), false, false);
  return fmaxf(__uint_as_float(s[0]), __uint_as_float(s[1]));
}
__device__ __forceinline__ float wave_sum(float x) {
  x += dpp<XOR1>(x); x += dpp<XOR2>(x); x += dpp<XOR7>(x); x += dpp<XOR8>(x);
  x = x16_sum(x); x = x32_sum(x); return x;
}
__device__ __forceinline__ float wave_max(float x) {
  x = fmaxf(x, dpp<XOR1>(x)); x = fmaxf(x, dpp<XOR2>(x)); x = fmaxf(x, dpp<XOR7>(x)); x = fmaxf(x, dpp<XOR8>(x));
  x = x16_max(x); x = x32_max(x); return x;
}
template <int CTRL, int BIT> __device__ __forceinline__ float trs(float lo, float hi, int lane) {
  const float t1 = lo + dpp<CTRL>(lo), t2 = hi + dpp<CTRL>(hi);
  return (lane & BIT) ? t2 : t1;
}
__device__ __forceinline__ void bar() { asm volatile("s_waitcnt lgkmcnt(0)\n\ts_barrier" ::: "memory"); }
#define SB() __builtin_amdgcn_sched_barrier(0)
#define PIN8(a_) asm volatile("" : "+v"(a_[0]), "+v"(a_[1]), "+v"(a_[2]), "+v"(a_[3]), "+v"(a_[4]), "+v"(a_[5]), "+v"(a_[6]), "+v"(a_[7]))
#define PIN1(a_) asm volatile("" : "+v"(a_))
#define MKRS(ptr_, bytes_) __builtin_amdgcn_make_buffer_rsrc((void*)(ptr_), 0, (bytes_), 0x00020000)
#define STAMP(i_)

__device__ __forceinline__ u32x4 mkdesc(const void* p, unsigned bytes) {
  const unsigned long long a = (unsigned long long)p;
  u32x4 d;
  d[0] = __builtin_amdgcn_readfirstlane((unsigned)a);
  d[1] = __builtin_amdgcn_readfirstlane((unsigned)(a >> 32) & 0xffffu);
  d[2] = __builtin_amdgcn_readfirstlane(bytes);
  d[3] = 0x00020000u;
  return d;
}
#define K_POL " nt"
#define V_POL " nt"
template <int OFF> __device__ __forceinline__ void ld_nt_new(u32x4& d, int voff, const u32x4& rsrc, int soff) {
  asm volatile("buffer_load_dwordx4 %0, %1, %2, %3 offen offset:%4" K_POL : "=v"(d) : "v"(voff), "s"(rsrc), "s"(soff), "i"(OFF));
}
template <int OFF> __device__ __forceinline__ void ld_nt_re(u32x4& d, int voff, const u32x4& rsrc, int soff) {
  asm volatile("buffer_load_dwordx4 %0, %1, %2, %3 offen offset:%4" K_POL : "+v"(d) : "v"(voff), "s"(rsrc), "s"(soff), "i"(OFF));
}
template <int OFF> __device__ __forceinline__ void ld_v_re(u32x4& d, int voff, const u32x4& rsrc, int soff) {
  asm volatile("buffer_load_dwordx4 %0, %1, %2, %3 offen offset:%4" V_POL : "+v"(d) : "v"(voff), "s"(rsrc), "s"(soff), "i"(OFF));
}
#define WO_POL ""
#define W_POL ""
#define C_POL ""
template <int OFF> __device__ __forceinline__ void ld_pl_re(u32x4& d, int voff, const u32x4& rsrc, int soff) {
  asm volatile("buffer_load_dwordx4 %0, %1, %2, %3 offen offset:%4" WO_POL : "+v"(d) : "v"(voff), "s"(rsrc), "s"(soff), "i"(OFF));
}
__device__ __forceinline__ void ld_g(u32x4& d, const void* p) {
  asm volatile("global_load_dwordx4 %0, %1, off" W_POL : "=v"(d) : "v"(p));
}
__device__ __forceinline__ void ld_b(u32x4& d, int voff, const u32x4& rsrc, int soff) {
  asm volatile("buffer_load_dwordx4 %0, %1, %2, %3 offen" C_POL : "=v"(d) : "v"(voff), "s"(rsrc), "s"(soff));
}
__device__ __forceinline__ void ld_sc1(u32x4& d, int voff, const u32x4& rsrc) {
  asm volatile("buffer_load_dwordx4 %0, %1, %2, 0 offen sc1" : "=v"(d) : "v"(voff), "s"(rsrc) : "memory");
}
template <int N> __device__ __forceinline__ void waitv(u32x4& d) { asm volatile("s_waitcnt vmcnt(%1)" : "+v"(d) : "i"(N)); }

struct Ctx {
  u32x4 rk, rv, ro;
  int lo16, wvo;
};
template <int n> __device__ __forceinline__ void issue(u32x4 (&R)[WK], const Ctx& c) {
  if constexpr (n < NISS) {
    constexpr int sl = slot_of(n);
    if constexpr (n < WK) {
      if constexpr (n >= KA) ld_nt_new<(n & 3) * 1024>(R[sl], c.lo16, c.rk, (n >> 2) * 32768);
      else ld_nt_re<(n & 3) * 1024>(R[sl], c.lo16, c.rk, (n >> 2) * 32768);
    } else if constexpr (is_wo_step(n - WK)) {
      constexpr int k = nwo_before(n - WK);
      ld_pl_re<(k & 3) * 896>(R[sl], c.wvo, c.ro, (k >> 2) * 3584);
    } else {
      constexpr int m = kv_of_step(n - WK);
      if constexpr (m < 64) ld_nt_re<(m & 3) * 1024>(R[sl], c.lo16, c.rk, (m >> 2) * 32768);
      else ld_v_re<((m - 64) & 3) * 1024>(R[sl], c.lo16, c.rv, ((m - 64) >> 2) * 32768);
    }
  }
}
template <int A, int... I> __device__ __forceinline__ void issue_range(u32x4 (&R)[WK], const Ctx& c, std::integer_sequence<int, I...>) {
  (issue<A + I>(R, c), ...);
}

struct KState { float t0, t1, t2, t3, t4; float sc[2]; };
template <int c> __device__ __forceinline__ void kstep(u32x4 (&R)[WK], const Ctx& cx, const u32x4& q4, KState& st, int lane) {
  constexpr int sl = slot_of(idx_of_step(c));
  waitv<wait_of_step(c)>(R[sl]);
  float d = dot2(R[sl][0], q4[0], 0.f);
  d = dot2(R[sl][1], q4[1], d);
  d = dot2(R[sl][2], q4[2], d);
  d = dot2(R[sl][3], q4[3], d);
  PIN1(d);
  issue<WK + c>(R, cx);
  if constexpr (BARN && (c % BARN) == BARN - 1) __builtin_amdgcn_s_barrier();
  constexpr int i = c & 31;
  if constexpr ((i & 1) == 0) { st.t0 = d; return; }
  else {
    d = sw16(st.t0, d);
    if constexpr ((i & 2) == 0) { st.t1 = d; return; }
    else {
      d = trs<XOR8, 8>(st.t1, d, lane);
      if constexpr ((i & 4) == 0) { st.t2 = d; return; }
      else {
        d = trs<XOR7, 4>(st.t2, d, lane);
        if constexpr ((i & 8) == 0) { st.t3 = d; return; }
        else {
          d = trs<XOR2, 2>(st.t3, d, lane);
          if constexpr ((i & 16) == 0) { st.t4 = d; return; }
          else {
            d = trs<XOR1, 1>(st.t4, d, lane);
            st.sc[c >> 5] = rbf(d) * 0.0625f;
          }
        }
      }
    }
  }
}
template <int... I> __device__ __forceinline__ void kphase(u32x4 (&R)[WK], const Ctx& cx, const u32x4& q4, KState& st, int lane, std::integer_sequence<int, I...>) {
  (kstep<I>(R, cx, q4, st, lane), ...);
}

struct VState { float acc[8]; f32x4 p4; u32x4 mlg, zg; };
constexpr int GATHER_STEP = 40;
template <int j> __device__ __forceinline__ void vstep(u32x4 (&R)[WK], const Ctx& cx, VState& st, const float* pl, const u32x4& rws, int ml_off, int z_off) {
  constexpr int c = 64 + j;
  constexpr int sl = slot_of(idx_of_step(c));
  if constexpr ((j & 3) == 0) st.p4 = *reinterpret_cast<const f32x4*>(pl + j);
  waitv<wait_of_step(c)>(R[sl]);
  const float p = st.p4[j & 3];
#pragma unroll
  for (int k = 0; k < 4; ++k) {
    st.acc[2 * k] = fmaf(p, bflo(R[sl][k]), st.acc[2 * k]);
    st.acc[2 * k + 1] = fmaf(p, bfhi(R[sl][k]), st.acc[2 * k + 1]);
  }
  PIN8(st.acc);
  issue<WK + c>(R, cx);
  if constexpr (j == GATHER_STEP) { ld_sc1(st.mlg, ml_off, rws); ld_sc1(st.zg, z_off, rws); }
  if constexpr (j >= 48) {
    constexpr int i = j - 48;
    static_assert(idx_of_kv(64 + j) > idx_of_wo(2 * i + 1), "wo pair landed");
    constexpr int sa = slot_of(idx_of_wo(2 * i)), sb = slot_of(idx_of_wo(2 * i + 1));
    PIN1(R[sa]); PIN1(R[sb]);
#pragma unroll
    for (int k = 0; k < 4; ++k) {
      const unsigned a = R[sa][k], e = R[sb][k];
      R[sa][k] = perm_lo(a, e); R[sb][k] = perm_hi(a, e);
    }
    PIN1(R[sa]); PIN1(R[sb]);
  }
  if constexpr (BARN && (j % BARN) == BARN - 1 && j < 48) __builtin_amdgcn_s_barrier();
}
template <int... I> __device__ __forceinline__ void vphase(u32x4 (&R)[WK], const Ctx& cx, VState& st, const float* pl, const u32x4& rws, int ml_off, int z_off, std::integer_sequence<int, I...>) {
  (vstep<I>(R, cx, st, pl, rws, ml_off, z_off), ...);
}
template <int i> __device__ __forceinline__ void ostep(u32x4 (&R)[WK], unsigned opk, float (&ya)[8]) {
  constexpr int sa = slot_of(idx_of_wo(2 * i)), sb = slot_of(idx_of_wo(2 * i + 1));
  const unsigned so = __builtin_amdgcn_readlane(opk, 2 * i);
#pragma unroll
  for (int k = 0; k < 4; ++k) {
    ya[2 * k] = dot2(R[sa][k], so, ya[2 * k]);
    ya[2 * k + 1] = dot2(R[sb][k], so, ya[2 * k + 1]);
  }
}
template <int... I> __device__ __forceinline__ void ophase(u32x4 (&R)[WK], unsigned opk, float (&ya)[8], std::integer_sequence<int, I...>) {
  (ostep<I>(R, opk, ya), ...);
}
static_assert(WK > 32 && KA <= WK, "plan");

__device__ __forceinline__ f32x2 gemv8(const u32x4 (&F)[8], const unsigned (&xp)[4]) {
  float fa[8];
#pragma unroll
  for (int k = 0; k < 8; ++k) fa[k] = 0.f;
#pragma unroll
  for (int i = 0; i < 4; ++i)
#pragma unroll
    for (int k = 0; k < 4; ++k) {
      fa[2 * k] = dot2(perm_lo(F[2 * i][k], F[2 * i + 1][k]), xp[i], fa[2 * k]);
      fa[2 * k + 1] = dot2(perm_hi(F[2 * i][k], F[2 * i + 1][k]), xp[i], fa[2 * k + 1]);
    }
  float f4[4];
  f32x2 f2;
#pragma unroll
  for (int k = 0; k < 4; ++k) f4[k] = sw32(fa[k], fa[k + 4]);
#pragma unroll
  for (int k = 0; k < 2; ++k) { float v = sw16(f4[k], f4[k + 2]); v += dpp<ROR4>(v); v += dpp<XOR8>(v); f2[k] = v; }
  return f2;
}

__global__ void __launch_bounds__(NTHR, 2)
fused_kernel(const u16* __restrict__ x, const u16* __restrict__ kc, const u16* __restrict__ vc,
             const u16* __restrict__ wq, const u16* __restrict__ wo, const u16* __restrict__ wg,
             const u16* __restrict__ wu, const u16* __restrict__ wd, u16* __restrict__ out,
             unsigned char* ws) {
  __shared__ __attribute__((aligned(16))) float lds[L_END];
  unsigned* ldsu = reinterpret_cast<unsigned*>(lds);
  const int t = threadIdx.x, lane = t & 63;
  const int w = __builtin_amdgcn_readfirstlane(t >> 6);
  const int g = blockIdx.x;
  const int xcd = g & 7, r = g >> 3, hsel = (r >> 3) & 1, b = r >> 4, s = r & 7;
  const int h = xcd + 8 * hsel;
  const int i16 = hsel * 8 + s;
  const int pidx = h * 8 + s;
  const bool has_ffn = i16 < 14;
  const int slice = xcd * 14 + (has_ffn ? i16 : 13);
  const int zrow = has_ffn ? slice : (112 + xcd * 2 + (i16 - 14));
  const bool is_cons = i16 >= 9;
  const int ci = xcd * 7 + (is_cons ? (i16 - 9) : 0);
  const int half = lane >> 5, l32 = lane & 31;
  const int l56 = lane < 56 ? lane : lane - 8;

  const __amdgpu_buffer_rsrc_t rs = MKRS(ws, WS_END);
  const u32x4 rws = mkdesc(ws, WS_END);
  const size_t kvoff = ((size_t)(b * H_ + h) * SK + s * KEYS_WG + w * 8) * DH;
  Ctx cx;
  cx.rk = mkdesc(kc + kvoff, 524288 - w * 4096);
  cx.rv = mkdesc(vc + kvoff, 524288 - w * 4096);
  cx.ro = mkdesc(wo + (size_t)(h * 256 + w * 32) * D_, 32 * 896);
  cx.lo16 = lane * 16;
  cx.wvo = l56 * 16;
  const u32x4 rcode = mkdesc((const void*)(__builtin_amdgcn_s_getpc() & ~63ull), w == 7 ? CODEPF : 0);

  STAMP(0);
  u32x4 R[WK];
#pragma unroll
  for (int i = 0; i < KA; ++i) asm volatile("" : "=v"(R[i]));
  u32x4 ep;
  asm volatile("s_nop 4");
  ld_sc1(ep, WS_EPOCH + b * 256, rws);
  u32x4 cpf;
  ld_b(cpf, cx.lo16, rcode, r * 1024);
  u32x4 xv, Fq[8], Fg[8], Fd[4];
  const bool worker = (w >= 1 && w <= 4);
  const int tw = t - 64;
  const int mh = t & 3, j0 = (tw >> 2) & 63;
  const int j3 = (j0 < 32) ? (j0 + 192) : j0;
  if (w == 0) {
    if (lane == 0) ldsu[L_CNT] = 0u;
    issue_range<0>(R, cx, std::make_integer_sequence<int, KA>{});
  } else if (worker) {
    ld_g(xv, x + b * D_ + l56 * 8);
    const u16* qb = wq + h * 256 + s * 32 + mh * 8;
#pragma unroll
    for (int i = 0; i < 3; ++i) {
      ld_g(Fq[2 * i], qb + (size_t)(2 * (j0 + 64 * i)) * 4096);
      ld_g(Fq[2 * i + 1], qb + (size_t)(2 * (j0 + 64 * i) + 1) * 4096);
    }
    ld_g(Fq[6], qb + (size_t)(2 * j3) * 4096);
    ld_g(Fq[7], qb + (size_t)(2 * j3 + 1) * 4096);
    const u16* base = ((mh & 2) ? wu : wg) + slice * 16 + (mh & 1) * 8;
#pragma unroll
    for (int i = 0; i < 3; ++i) {
      ld_g(Fg[2 * i], base + (size_t)(2 * (j0 + 64 * i)) * F_);
      ld_g(Fg[2 * i + 1], base + (size_t)(2 * (j0 + 64 * i) + 1) * F_);
    }
    ld_g(Fg[6], base + (size_t)(2 * j3) * F_);
    ld_g(Fg[7], base + (size_t)(2 * j3 + 1) * F_);
    const u16* pd = wd + (size_t)(slice * 16 + 4 * (w - 1)) * D_ + l56 * 8;
#pragma unroll
    for (int i = 0; i < 4; ++i) ld_g(Fd[i], pd + i * D_);
  }
  bar();
  if (w == 0) {
    __builtin_amdgcn_s_setprio(3);
    waitv<KA>(ep);
    const unsigned tl0 = __builtin_amdgcn_readfirstlane(ep[0]) + 1u;
    {
      unsigned spins = 0;
      while (__hip_atomic_load(ldsu + L_CNT, __ATOMIC_RELAXED, __HIP_MEMORY_SCOPE_WORKGROUP) != 4u && ++spins < (1u << 22)) __builtin_amdgcn_s_sleep(1);
      asm volatile("" ::: "memory");
    }
    STAMP(2);
    if (lane < 8) {
      f32x4 a = {0.f, 0.f, 0.f, 0.f};
#pragma unroll
      for (int ww = 0; ww < 4; ++ww) a += *reinterpret_cast<const f32x4*>(lds + L_FRED + ww * 64 + 32 + lane * 4);
      u32x4 gr = {pk_bf16(a[0], a[1]), tl0, pk_bf16(a[2], a[3]), tl0};
      __builtin_amdgcn_raw_buffer_store_b128(gr, rs, WS_Q + (b * H_ + h) * 1024 + s * 128 + lane * 16, 0, 16);
    }
    const int q_off = WS_Q + (b * H_ + h) * 1024 + lane * 16;
    u32x4 qg;
    unsigned spins = 0;
    do {
      ld_sc1(qg, q_off, rws);
      waitv<0>(qg);
    } while (!__all(qg[1] == tl0 && qg[3] == tl0) && ++spins < (1u << 18));
    *reinterpret_cast<u32x2*>(ldsu + L_Q + lane * 2) = u32x2{qg[0], qg[2]};
    __builtin_amdgcn_s_setprio(0);
    STAMP(3);
  } else if (worker) {
    waitv<20>(xv);
    {
      float xf[8];
#pragma unroll
      for (int k = 0; k < 4; ++k) { xf[2 * k] = bflo(xv[k]); xf[2 * k + 1] = bfhi(xv[k]); }
      float sm = 0.f;
#pragma unroll
      for (int k = 0; k < 8; ++k) sm += xf[k];
      sm = lane < 56 ? sm : 0.f;
      sm = wave_sum(sm);
      const float mexact = sm / (float)D_;
      const float m = rbf(mexact);
      float vs = 0.f;
#pragma unroll
      for (int k = 0; k < 8; ++k) { float d = xf[k] - mexact; vs += d * d; }
      vs = lane < 56 ? vs : 0.f;
      vs = wave_sum(vs);
      const float var = rbf(vs / (float)D_);
      const float den = rbf(sqrtf(rbf(var + 1e-5f)));
      u32x4 xn;
#pragma unroll
      for (int k = 0; k < 4; ++k) xn[k] = pk_bf16(rbf(xf[2 * k] - m) / den, rbf(xf[2 * k + 1] - m) / den);
      if (lane < 56) {
        *reinterpret_cast<u32x4*>(ldsu + L_XN + w * 256 + lane * 4) = xn;
        if (w == 1) *reinterpret_cast<u32x4*>(ldsu + L_X + lane * 4) = xv;
      }
    }
    STAMP(1);
    unsigned xp[4];
#pragma unroll
    for (int i = 0; i < 3; ++i) xp[i] = ldsu[L_XN + w * 256 + j0 + 64 * i];
    xp[3] = ldsu[L_XN + w * 256 + j3];
    xp[3] = (j0 < 32) ? xp[3] : 0u;
    const int fo = L_FRED + (w - 1) * 64 + mh * 8 + (lane >> 5) * 4 + ((lane >> 4) & 1) * 2;
    asm volatile("s_waitcnt vmcnt(12)" : "+v"(Fq[0]), "+v"(Fq[1]), "+v"(Fq[2]), "+v"(Fq[3]), "+v"(Fq[4]), "+v"(Fq[5]), "+v"(Fq[6]), "+v"(Fq[7]));
    const f32x2 q2 = gemv8(Fq, xp);
    if ((lane & 12) == 0) *reinterpret_cast<f32x2*>(lds + fo + 32) = q2;
    asm volatile("s_waitcnt vmcnt(4)" : "+v"(Fg[0]), "+v"(Fg[1]), "+v"(Fg[2]), "+v"(Fg[3]), "+v"(Fg[4]), "+v"(Fg[5]), "+v"(Fg[6]), "+v"(Fg[7]));
    const f32x2 f2 = gemv8(Fg, xp);
    if ((lane & 12) == 0) *reinterpret_cast<f32x2*>(lds + fo) = f2;
    asm volatile("s_waitcnt lgkmcnt(0)" ::: "memory");
    if (lane == 0) __hip_atomic_fetch_add(ldsu + L_CNT, 1u, __ATOMIC_RELAXED, __HIP_MEMORY_SCOPE_WORKGROUP);
    STAMP(2);
    {
      unsigned spins = 0;
      while (__hip_atomic_load(ldsu + L_CNT, __ATOMIC_RELAXED, __HIP_MEMORY_SCOPE_WORKGROUP) != 4u && ++spins < (1u << 22)) __builtin_amdgcn_s_sleep(1);
      asm volatile("" ::: "memory");
    }
    f32x4 gg = {0.f, 0.f, 0.f, 0.f}, uu = {0.f, 0.f, 0.f, 0.f};
#pragma unroll
    for (int ww = 0; ww < 4; ++ww) {
      gg += *reinterpret_cast<const f32x4*>(lds + L_FRED + ww * 64 + 4 * (w - 1));
      uu += *reinterpret_cast<const f32x4*>(lds + L_FRED + ww * 64 + 16 + 4 * (w - 1));
    }
    float hm[4];
#pragma unroll
    for (int k = 0; k < 4; ++k) {
      const float gv = rbf(gg[k]), uv = rbf(uu[k]);
      const float sg = rbf(gv / (1.f + __expf(-gv)));
      hm[k] = sg * uv;
    }
    const unsigned hp0 = pk_bf16(hm[0], hm[1]), hp1 = pk_bf16(hm[2], hm[3]);
    asm volatile("s_waitcnt vmcnt(0)" : "+v"(Fd[0]), "+v"(Fd[1]), "+v"(Fd[2]), "+v"(Fd[3]));
    f32x4 z0, z1;
#pragma unroll
    for (int k = 0; k < 2; ++k) {
      z0[2 * k] = dot2(perm_lo(Fd[2][k], Fd[3][k]), hp1, dot2(perm_lo(Fd[0][k], Fd[1][k]), hp0, 0.f));
      z0[2 * k + 1] = dot2(perm_hi(Fd[2][k], Fd[3][k]), hp1, dot2(perm_hi(Fd[0][k], Fd[1][k]), hp0, 0.f));
      z1[2 * k] = dot2(perm_lo(Fd[2][k + 2], Fd[3][k + 2]), hp1, dot2(perm_lo(Fd[0][k + 2], Fd[1][k + 2]), hp0, 0.f));
      z1[2 * k + 1] = dot2(perm_hi(Fd[2][k + 2], Fd[3][k + 2]), hp1, dot2(perm_hi(Fd[0][k + 2], Fd[1][k + 2]), hp0, 0.f));
    }
    if (lane < 56) {
      *reinterpret_cast<f32x4*>(lds + L_YRED + (w - 1) * 448 + lane * 8) = z0;
      *reinterpret_cast<f32x4*>(lds + L_YRED + (w - 1) * 448 + lane * 8 + 4) = z1;
    }
    STAMP(3);
  }
  if (w != 0) issue_range<0>(R, cx, std::make_integer_sequence<int, KA>{});
  asm volatile("" ::: "memory");
  if (w == 0) bar();
  issue_range<KA>(R, cx, std::make_integer_sequence<int, WK - KA>{});
  if (w != 0) bar();
  waitv<WK - KA>(ep);
  PIN1(cpf);
  const unsigned long long e64 = (((unsigned long long)ep[1] << 32) | ep[0]) + 1ull;
  const unsigned tlo = __builtin_amdgcn_readfirstlane((unsigned)e64);
  const unsigned thi_full = __builtin_amdgcn_readfirstlane((unsigned)(e64 >> 32));
  const unsigned thi = thi_full & 0xffu;
  const unsigned g0tag = tlo << 24, g1tag = (tlo >> 8) | (thi << 24);
  const u32x4 q4 = *reinterpret_cast<const u32x4*>(ldsu + L_Q + l32 * 4);
  if (t < 224) {
    f32x2 zz = {0.f, 0.f};
#pragma unroll
    for (int ww = 0; ww < 4; ++ww) zz += *reinterpret_cast<const f32x2*>(lds + L_YRED + ww * 448 + 2 * t);
    const unsigned d0 = (__float_as_uint(zz[0]) + 0x80u) >> 8, d1 = (__float_as_uint(zz[1]) + 0x80u) >> 8;
    u32x4 gr = {d0 | g0tag, g1tag, d1 | g0tag, g1tag};
    __builtin_amdgcn_raw_buffer_store_b128(gr, rs, WS_Z + ((b * 56 + (t >> 2)) * 128 + zrow) * 64 + (t & 3) * 16, 0, 16);
  }
  STAMP(4);

  KState ks;
  kphase(R, cx, q4, ks, lane, std::make_integer_sequence<int, 64>{});
  STAMP(5);
  const float mw = wave_max(fmaxf(ks.sc[0], ks.sc[1]));
  {
    const float p0 = __expf(ks.sc[0] - mw), p1 = __expf(ks.sc[1] - mw);
    const float lw = wave_sum(p0 + p1);
    const int li = ((lane >> 4) & 1) | ((lane >> 2) & 2) | (lane & 4) | ((lane << 2) & 8) | ((lane << 4) & 16);
    lds[L_P + w * 128 + half * 64 + li] = p0;
    lds[L_P + w * 128 + half * 64 + 32 + li] = p1;
    if (lane == 0) {
      u32x4 gr = {__float_as_uint(mw), tlo, __float_as_uint(lw), tlo};
      __builtin_amdgcn_raw_buffer_store_b128(gr, rs, WS_ML + ((b * H_ + h) * 64 + s * 8 + w) * 16, 0, 16);
    }
  }

  VState vs;
#pragma unroll
  for (int k = 0; k < 8; ++k) vs.acc[k] = 0.f;
  const int ml_off = WS_ML + ((b * H_ + h) * 64 + lane) * 16;
  const int z_off = WS_Z + ((b * 56 + ci) * 128) * 64 + t * 16;
  vphase(R, cx, vs, lds + L_P + w * 128 + half * 64, rws, ml_off, z_off, std::make_integer_sequence<int, 64>{});
  asm volatile("s_waitcnt vmcnt(0)" : "+v"(vs.mlg), "+v"(vs.zg));
  STAMP(6);
  {
    unsigned spins = 0;
    while (!__all(vs.mlg[1] == tlo && vs.mlg[3] == tlo) && ++spins < (1u << 18)) {
      __builtin_amdgcn_s_sleep(1);
      ld_sc1(vs.mlg, ml_off, rws);
      waitv<0>(vs.mlg);
    }
  }
  {
    const float mi = __uint_as_float(vs.mlg[0]), li = __uint_as_float(vs.mlg[2]);
    const float M = wave_max(mi);
    const float L = wave_sum(li * __expf(mi - M));
    const float f = __expf(mw - M) / L;
    f32x4 oh;
#pragma unroll
    for (int k = 0; k < 4; ++k) oh[k] = sw32(vs.acc[k], vs.acc[k + 4]) * f;
    *reinterpret_cast<f32x4*>(lds + L_ORED + w * 256 + l32 * 8 + half * 4) = oh;
  }
  STAMP(7);
  bar();
  STAMP(8);
  {
    float ov = 0.f;
#pragma unroll
    for (int ww = 0; ww < 8; ++ww) ov += lds[L_ORED + ww * 256 + w * 32 + l32];
    const float on = dpp<XOR1>(ov);
    const unsigned opk = pk_bf16(ov, on);
    float ya[8];
#pragma unroll
    for (int k = 0; k < 8; ++k) ya[k] = 0.f;
    ophase(R, opk, ya, std::make_integer_sequence<int, 16>{});
    if (lane < 56) {
      *reinterpret_cast<f32x4*>(lds + L_YRED + w * 448 + lane * 8) = f32x4{ya[0], ya[1], ya[2], ya[3]};
      *reinterpret_cast<f32x4*>(lds + L_YRED + w * 448 + lane * 8 + 4) = f32x4{ya[4], ya[5], ya[6], ya[7]};
    }
  }
  bar();
  if (t < 224) {
    f32x2 yy = {0.f, 0.f};
#pragma unroll
    for (int ww = 0; ww < 8; ++ww) yy += *reinterpret_cast<const f32x2*>(lds + L_YRED + ww * 448 + 2 * t);
    const unsigned d0 = (__float_as_uint(yy[0]) + 0x80u) >> 8, d1 = (__float_as_uint(yy[1]) + 0x80u) >> 8;
    u32x4 gr = {d0 | g0tag, g1tag, d1 | g0tag, g1tag};
    __builtin_amdgcn_raw_buffer_store_b128(gr, rs, WS_Y + ((b * 56 + (t >> 2)) * 128 + pidx) * 64 + (t & 3) * 16, 0, 16);
  }
  STAMP(9);
  if (!is_cons) return;

  {
    const int y_off = WS_Y + ((b * 56 + ci) * 128) * 64 + t * 16;
    u32x4 yg;
    ld_sc1(yg, y_off, rws);
    const bool zlive = t < 448;
    u32x4 zg = vs.zg;
    unsigned spins = 0;
    while (!__all(!zlive || ((zg[0] & 0xff000000u) == g0tag && zg[1] == g1tag && (zg[2] & 0xff000000u) == g0tag && zg[3] == g1tag)) && ++spins < (1u << 18)) {
      __builtin_amdgcn_s_sleep(1);
      ld_sc1(zg, z_off, rws);
      waitv<0>(zg);
    }
    float z0 = zlive ? __uint_as_float(zg[0] << 8) : 0.f, z1 = zlive ? __uint_as_float(zg[2] << 8) : 0.f;
    z0 += dpp<ROR4>(z0); z0 += dpp<XOR8>(z0); z0 = x16_sum(z0); z0 = x32_sum(z0);
    z1 += dpp<ROR4>(z1); z1 += dpp<XOR8>(z1); z1 = x16_sum(z1); z1 = x32_sum(z1);
    PIN1(z0); PIN1(z1);
    waitv<0>(yg);
    spins = 0;
    while (!__all((yg[0] & 0xff000000u) == g0tag && yg[1] == g1tag && (yg[2] & 0xff000000u) == g0tag && yg[3] == g1tag) && ++spins < (1u << 18)) {
      ld_sc1(yg, y_off, rws);
      waitv<0>(yg);
    }
    float y0 = __uint_as_float(yg[0] << 8), y1 = __uint_as_float(yg[2] << 8);
    y0 += dpp<ROR4>(y0); y0 += dpp<XOR8>(y0); y0 = x16_sum(y0); y0 = x32_sum(y0);
    y1 += dpp<ROR4>(y1); y1 += dpp<XOR8>(y1); y1 = x16_sum(y1); y1 = x32_sum(y1);
    if (lane < 4) {
      *reinterpret_cast<f32x2*>(lds + L_C + w * 16 + lane * 2) = f32x2{y0, y1};
      *reinterpret_cast<f32x2*>(lds + L_C + w * 16 + 8 + lane * 2) = f32x2{z0, z1};
    }
    STAMP(10);
    bar();
    if (t < 8) {
      float a = 0.f, f = 0.f;
#pragma unroll
      for (int ww = 0; ww < 8; ++ww) { a += lds[L_C + ww * 16 + t]; f += lds[L_C + ww * 16 + 8 + t]; }
      const unsigned xo = reinterpret_cast<const u16*>(ldsu + L_X)[ci * 8 + t];
      const float xr = __uint_as_float(xo << 16);
      const float res = rbf(xr + rbf(a)) + rbf(f);
      out[b * D_ + ci * 8 + t] = __builtin_bit_cast(u16, (__bf16)res);
    }
    STAMP(11);
    if (ci == 0 && t == 0) {
      u32x2 ne = {tlo, thi_full};
      __builtin_amdgcn_raw_buffer_store_b64(ne, rs, WS_EPOCH + b * 256, 0, 16);
    }
  }
}

extern "C" void kernel_launch(void* const* d_in, const int* in_sizes, int n_in,
                              void* d_out, int out_size, void* d_ws, size_t ws_size,
                              hipStream_t stream) {
  const u16* x = (const u16*)d_in[0];
  const u16* k = (const u16*)d_in[1];
  const u16* v = (const u16*)d_in[2];
  const u16* wq = (const u16*)d_in[3];
  const u16* wo = (const u16*)d_in[4];
  const u16* wg = (const u16*)d_in[6];
  const u16* wu = (const u16*)d_in[7];
  const u16* wd = (const u16*)d_in[8];
  fused_kernel<<<NWG, NTHR, 0, stream>>>(x, k, v, wq, wo, wg, wu, wd, (u16*)d_out, (unsigned char*)d_ws);
}
```

```cpp
#include <hip/hip_runtime.h>
#include <hip/hip_bf16.h>
#include <stdint.h>
#include <utility>
#define NOTOUCH 1

typedef unsigned short u16;
typedef unsigned u32x4 __attribute__((ext_vector_type(4)));
typedef unsigned u32x2 __attribute__((ext_vector_type(2)));
typedef float f32x4 __attribute__((ext_vector_type(4)));
typedef float f32x2 __attribute__((ext_vector_type(2)));
typedef __bf16 bf16x2 __attribute__((ext_vector_type(2)));

constexpr int D_ = 448, H_ = 16, SK = 8192, DH = 256, F_ = 1792;
constexpr int NWG = 256, NTHR = 512;
constexpr int NSPLIT = 8;
constexpr int KEYS_WG = SK / NSPLIT;
#define WKK 48
constexpr int WK = WKK;
#define KAA 16
#define KA0 16
constexpr int KA = KAA;
#define VAA 48
constexpr int VA = VAA;
#define BARN 8
#define CODEPF 23552
#define WOP 3
#define WOS 0
constexpr int NISS = 160;
constexpr int nwo_before(int c) { return c <= WOS ? 0 : ((c - WOS) / WOP < 32 ? (c - WOS) / WOP : 32); }
constexpr bool is_wo_step(int c) { return c >= WOS && ((c - WOS) % WOP) == WOP - 1 && (c - WOS) / WOP < 32; }
constexpr int kv_of_step(int c) { return WK + c - nwo_before(c); }
constexpr int idx_of_kv(int m) {
  if (m < WK) return m;
  for (int c = 0; c < NISS; ++c) if (!is_wo_step(c) && kv_of_step(c) == m) return WK + c;
  return -1;
}
constexpr int idx_of_wo(int k) { return WK + WOS + k * WOP + WOP - 1; }
constexpr int idx_of_step(int c) { return idx_of_kv(c); }
constexpr int slot_of(int n) { return n < WK ? n : slot_of(idx_of_step(n - WK)); }
constexpr int wait_of_step(int c) { return ((WK + c < NISS) ? WK + c : NISS) - idx_of_step(c) - 1; }
static_assert(idx_of_kv(127) < NISS && idx_of_kv(127) > 0 && idx_of_wo(31) < NISS, "plan");
static_assert(idx_of_kv(112) > idx_of_wo(1) && idx_of_kv(127) > idx_of_wo(31) && idx_of_kv(120) > idx_of_wo(17), "wo landed before its pre-permute");

constexpr int WS_EPOCH = 0;
constexpr int WS_ML = 4096;
constexpr int WS_Q = WS_ML + 2 * 16 * 64 * 16;
constexpr int WS_Y = WS_Q + 2 * 16 * 1024;
constexpr int WS_Z = WS_Y + 2 * 56 * 128 * 64;
constexpr int WS_END = WS_Z + 2 * 56 * 128 * 64;

constexpr int L_XN = 0;
constexpr int L_ORED = L_XN + 8 * 256;
constexpr int L_FRED = L_ORED + 8 * 256;
constexpr int L_YRED = L_FRED + 8 * 64;
constexpr int L_P = L_YRED + 8 * 448;
constexpr int L_C = L_P + 8 * 128;
constexpr int L_X = L_C + 8 * 16;
constexpr int L_Q = L_X + 256;
constexpr int L_CNT = L_Q + 128;
constexpr int L_END = L_CNT + 4;

__device__ __forceinline__ float dot2(unsigned a, unsigned b, float c) {
  return __builtin_amdgcn_fdot2_f32_bf16(__builtin_bit_cast(bf16x2, a), __builtin_bit_cast(bf16x2, b), c, false);
}
__device__ __forceinline__ float bflo(unsigned v) { return __uint_as_float(v << 16); }
__device__ __forceinline__ float bfhi(unsigned v) { return __uint_as_float(v & 0xffff0000u); }
__device__ __forceinline__ unsigned pk_bf16(float lo, float hi) {
  bf16x2 r; r[0] = (__bf16)lo; r[1] = (__bf16)hi; return __builtin_bit_cast(unsigned, r);
}
__device__ __forceinline__ float rbf(float x) { return (float)(__bf16)x; }
__device__ __forceinline__ unsigned perm_lo(unsigned a, unsigned b) { return __builtin_amdgcn_perm(b, a, 0x05040100u); }
__device__ __forceinline__ unsigned perm_hi(unsigned a, unsigned b) { return __builtin_amdgcn_perm(b, a, 0x07060302u); }
template <int CTRL> __device__ __forceinline__ float dpp(float x) {
  return __builtin_bit_cast(float, __builtin_amdgcn_mov_dpp(__builtin_bit_cast(int, x), CTRL, 0xf, 0xf, true));
}
constexpr int XOR1 = 0xB1, XOR2 = 0x4E, XOR7 = 0x141, XOR8 = 0x128, ROR4 = 0x124;
__device__ __forceinline__ float sw16(float a, float b) {
  auto s = __builtin_amdgcn_permlane16_swap(__float_as_uint(a), __float_as_uint(b), false, false);
  return __uint_as_float(s[0]) + __uint_as_float(s[1]);
}
__device__ __forceinline__ float sw32(float a, float b) {
  auto s = __builtin_amdgcn_permlane32_swap(__float_as_uint(a), __float_as_uint(b), false, false);
  return __uint_as_float(s[0]) + __uint_as_float(s[1]);
}
__device__ __forceinline__ float x16_sum(float x) { return sw16(x, x); }
__device__ __forceinline__ float x32_sum(float x) { return sw32(x, x); }
__device__ __forceinline__ float x16_max(float x) {
  auto s = __builtin_amdgcn_permlane16_swap(__float_as_uint(x), __float_as_uint(x), false, false);
  return fmaxf(__uint_as_float(s[0]), __uint_as_float(s[1]));
}
__device__ __forceinline__ float x32_max(float x) {
  auto s = __builtin_amdgcn_permlane32_swap(__float_as_uint(x), __float_as_uint(x), false, false);
  return fmaxf(__uint_as_float(s[0]), __uint_as_float(s[1]));
}
__device__ __forceinline__ float wave_sum(float x) {
  x += dpp<XOR1>(x); x += dpp<XOR2>(x); x += dpp<XOR7>(x); x += dpp<XOR8>(x);
  x = x16_sum(x); x = x32_sum(x); return x;
}
__device__ __forceinline__ float wave_max(float x) {
  x = fmaxf(x, dpp<XOR1>(x)); x = fmaxf(x, dpp<XOR2>(x)); x = fmaxf(x, dpp<XOR7>(x)); x = fmaxf(x, dpp<XOR8>(x));
  x = x16_max(x); x = x32_max(x); return x;
}
template <int CTRL, int BIT> __device__ __forceinline__ float trs(float lo, float hi, int lane) {
  const float t1 = lo + dpp<CTRL>(lo), t2 = hi + dpp<CTRL>(hi);
  return (lane & BIT) ? t2 : t1;
}
__device__ __forceinline__ void bar() { asm volatile("s_waitcnt lgkmcnt(0)\n\ts_barrier" ::: "memory"); }
#define SB() __builtin_amdgcn_sched_barrier(0)
#define PIN8(a_) asm volatile("" : "+v"(a_[0]), "+v"(a_[1]), "+v"(a_[2]), "+v"(a_[3]), "+v"(a_[4]), "+v"(a_[5]), "+v"(a_[6]), "+v"(a_[7]))
#define PIN1(a_) asm volatile("" : "+v"(a_))
#define MKRS(ptr_, bytes_) __builtin_amdgcn_make_buffer_rsrc((void*)(ptr_), 0, (bytes_), 0x00020000)
#define STAMP(i_)

__device__ __forceinline__ u32x4 mkdesc(const void* p, unsigned bytes) {
  const unsigned long long a = (unsigned long long)p;
  u32x4 d;
  d[0] = __builtin_amdgcn_readfirstlane((unsigned)a);
  d[1] = __builtin_amdgcn_readfirstlane((unsigned)(a >> 32) & 0xffffu);
  d[2] = __builtin_amdgcn_readfirstlane(bytes);
  d[3] = 0x00020000u;
  return d;
}
#define K_POL " nt"
#define V_POL " nt"
template <int OFF> __device__ __forceinline__ void ld_nt_new(u32x4& d, int voff, const u32x4& rsrc, int soff) {
  asm volatile("buffer_load_dwordx4 %0, %1, %2, %3 offen offset:%4" K_POL : "=v"(d) : "v"(voff), "s"(rsrc), "s"(soff), "i"(OFF));
}
template <int OFF> __device__ __forceinline__ void ld_nt_re(u32x4& d, int voff, const u32x4& rsrc, int soff) {
  asm volatile("buffer_load_dwordx4 %0, %1, %2, %3 offen offset:%4" K_POL : "+v"(d) : "v"(voff), "s"(rsrc), "s"(soff), "i"(OFF));
}
template <int OFF> __device__ __forceinline__ void ld_v_re(u32x4& d, int voff, const u32x4& rsrc, int soff) {
  asm volatile("buffer_load_dwordx4 %0, %1, %2, %3 offen offset:%4" V_POL : "+v"(d) : "v"(voff), "s"(rsrc), "s"(soff), "i"(OFF));
}
#define WO_POL ""
#define W_POL ""
#define C_POL ""
template <int OFF> __device__ __forceinline__ void ld_pl_re(u32x4& d, int voff, const u32x4& rsrc, int soff) {
  asm volatile("buffer_load_dwordx4 %0, %1, %2, %3 offen offset:%4" WO_POL : "+v"(d) : "v"(voff), "s"(rsrc), "s"(soff), "i"(OFF));
}
__device__ __forceinline__ void ld_g(u32x4& d, const void* p) {
  asm volatile("global_load_dwordx4 %0, %1, off" W_POL : "=v"(d) : "v"(p));
}
__device__ __forceinline__ void ld_b(u32x4& d, int voff, const u32x4& rsrc, int soff) {
  asm volatile("buffer_load_dwordx4 %0, %1, %2, %3 offen" C_POL : "=v"(d) : "v"(voff), "s"(rsrc), "s"(soff));
}
__device__ __forceinline__ void ld_sc1(u32x4& d, int voff, const u32x4& rsrc) {
  asm volatile("buffer_load_dwordx4 %0, %1, %2, 0 offen sc1" : "=v"(d) : "v"(voff), "s"(rsrc) : "memory");
}
template <int N> __device__ __forceinline__ void waitv(u32x4& d) { asm volatile("s_waitcnt vmcnt(%1)" : "+v"(d) : "i"(N)); }

struct Ctx {
  u32x4 rk, rv, ro;
  int lo16, wvo;
};
template <int n> __device__ __forceinline__ void issue(u32x4 (&R)[WK], const Ctx& c) {
  if constexpr (n < NISS) {
    constexpr int sl = slot_of(n);
    if constexpr (n < WK) {
      if constexpr (n >= KA) ld_nt_new<(n & 3) * 1024>(R[sl], c.lo16, c.rk, (n >> 2) * 32768);
      else ld_nt_re<(n & 3) * 1024>(R[sl], c.lo16, c.rk, (n >> 2) * 32768);
    } else if constexpr (is_wo_step(n - WK)) {
      constexpr int k = nwo_before(n - WK);
      ld_pl_re<(k & 3) * 896>(R[sl], c.wvo, c.ro, (k >> 2) * 3584);
    } else {
      constexpr int m = kv_of_step(n - WK);
      if constexpr (m < 64) ld_nt_re<(m & 3) * 1024>(R[sl], c.lo16, c.rk, (m >> 2) * 32768);
      else ld_v_re<((m - 64) & 3) * 1024>(R[sl], c.lo16, c.rv, ((m - 64) >> 2) * 32768);
    }
  }
}
template <int A, int... I> __device__ __forceinline__ void issue_range(u32x4 (&R)[WK], const Ctx& c, std::integer_sequence<int, I...>) {
  (issue<A + I>(R, c), ...);
}

struct KState { float t0, t1, t2, t3, t4; float sc[2]; };
template <int c> __device__ __forceinline__ void kstep(u32x4 (&R)[WK], const Ctx& cx, const u32x4& q4, KState& st, int lane) {
  constexpr int sl = slot_of(idx_of_step(c));
  waitv<wait_of_step(c)>(R[sl]);
  float d = dot2(R[sl][0], q4[0], 0.f);
  d = dot2(R[sl][1], q4[1], d);
  d = dot2(R[sl][2], q4[2], d);
  d = dot2(R[sl][3], q4[3], d);
  PIN1(d);
  issue<WK + c>(R, cx);
  if constexpr (BARN && (c % BARN) == BARN - 1) __builtin_amdgcn_s_barrier();
  constexpr int i = c & 31;
  if constexpr ((i & 1) == 0) { st.t0 = d; return; }
  else {
    d = sw16(st.t0, d);
    if constexpr ((i & 2) == 0) { st.t1 = d; return; }
    else {
      d = trs<XOR8, 8>(st.t1, d, lane);
      if constexpr ((i & 4) == 0) { st.t2 = d; return; }
      else {
        d = trs<XOR7, 4>(st.t2, d, lane);
        if constexpr ((i & 8) == 0) { st.t3 = d; return; }
        else {
          d = trs<XOR2, 2>(st.t3, d, lane);
          if constexpr ((i & 16) == 0) { st.t4 = d; return; }
          else {
            d = trs<XOR1, 1>(st.t4, d, lane);
            st.sc[c >> 5] = rbf(d) * 0.0625f;
          }
        }
      }
    }
  }
}
template <int... I> __device__ __forceinline__ void kphase(u32x4 (&R)[WK], const Ctx& cx, const u32x4& q4, KState& st, int lane, std::integer_sequence<int, I...>) {
  (kstep<I>(R, cx, q4, st, lane), ...);
}

struct VState { float acc[8]; f32x4 p4; u32x4 mlg, zg; };
constexpr int GATHER_STEP = 40;
template <int j> __device__ __forceinline__ void vstep(u32x4 (&R)[WK], const Ctx& cx, VState& st, const float* pl, const u32x4& rws, int ml_off, int z_off) {
  constexpr int c = 64 + j;
  constexpr int sl = slot_of(idx_of_step(c));
  if constexpr ((j & 3) == 0) st.p4 = *reinterpret_cast<const f32x4*>(pl + j);
  waitv<wait_of_step(c)>(R[sl]);
  const float p = st.p4[j & 3];
#pragma unroll
  for (int k = 0; k < 4; ++k) {
    st.acc[2 * k] = fmaf(p, bflo(R[sl][k]), st.acc[2 * k]);
    st.acc[2 * k + 1] = fmaf(p, bfhi(R[sl][k]), st.acc[2 * k + 1]);
  }
  PIN8(st.acc);
  issue<WK + c>(R, cx);
  if constexpr (j == GATHER_STEP) { ld_sc1(st.mlg, ml_off, rws); ld_sc1(st.zg, z_off, rws); }
  if constexpr (j >= 48) {
    constexpr int i = j - 48;
    static_assert(idx_of_kv(64 + j) > idx_of_wo(2 * i + 1), "wo pair landed");
    constexpr int sa = slot_of(idx_of_wo(2 * i)), sb = slot_of(idx_of_wo(2 * i + 1));
    PIN1(R[sa]); PIN1(R[sb]);
#pragma unroll
    for (int k = 0; k < 4; ++k) {
      const unsigned a = R[sa][k], e = R[sb][k];
      R[sa][k] = perm_lo(a, e); R[sb][k] = perm_hi(a, e);
    }
    PIN1(R[sa]); PIN1(R[sb]);
  }
  if constexpr (BARN && (j % BARN) == BARN - 1 && j < 48) __builtin_amdgcn_s_barrier();
}
template <int... I> __device__ __forceinline__ void vphase(u32x4 (&R)[WK], const Ctx& cx, VState& st, const float* pl, const u32x4& rws, int ml_off, int z_off, std::integer_sequence<int, I...>) {
  (vstep<I>(R, cx, st, pl, rws, ml_off, z_off), ...);
}
template <int i> __device__ __forceinline__ void ostep(u32x4 (&R)[WK], unsigned opk, float (&ya)[8]) {
  constexpr int sa = slot_of(idx_of_wo(2 * i)), sb = slot_of(idx_of_wo(2 * i + 1));
  const unsigned so = __builtin_amdgcn_readlane(opk, 2 * i);
#pragma unroll
  for (int k = 0; k < 4; ++k) {
    ya[2 * k] = dot2(R[sa][k], so, ya[2 * k]);
    ya[2 * k + 1] = dot2(R[sb][k], so, ya[2 * k + 1]);
  }
}
template <int... I> __device__ __forceinline__ void ophase(u32x4 (&R)[WK], unsigned opk, float (&ya)[8], std::integer_sequence<int, I...>) {
  (ostep<I>(R, opk, ya), ...);
}
static_assert(WK > 32 && KA <= WK, "plan");

__device__ __forceinline__ f32x2 gemv8(const u32x4 (&F)[8], const unsigned (&xp)[4]) {
  float fa[8];
#pragma unroll
  for (int k = 0; k < 8; ++k) fa[k] = 0.f;
#pragma unroll
  for (int i = 0; i < 4; ++i)
#pragma unroll
    for (int k = 0; k < 4; ++k) {
      fa[2 * k] = dot2(perm_lo(F[2 * i][k], F[2 * i + 1][k]), xp[i], fa[2 * k]);
      fa[2 * k + 1] = dot2(perm_hi(F[2 * i][k], F[2 * i + 1][k]), xp[i], fa[2 * k + 1]);
    }
  float f4[4];
  f32x2 f2;
#pragma unroll
  for (int k = 0; k < 4; ++k) f4[k] = sw32(fa[k], fa[k + 4]);
#pragma unroll
  for (int k = 0; k < 2; ++k) { float v = sw16(f4[k], f4[k + 2]); v += dpp<ROR4>(v); v += dpp<XOR8>(v); f2[k] = v; }
  return f2;
}

__global__ void __launch_bounds__(NTHR, 2)
fused_kernel(const u16* __restrict__ x, const u16* __restrict__ kc, const u16* __restrict__ vc,
             const u16* __restrict__ wq, const u16* __restrict__ wo, const u16* __restrict__ wg,
             const u16* __restrict__ wu, const u16* __restrict__ wd, u16* __restrict__ out,
             unsigned char* ws) {
  __shared__ __attribute__((aligned(16))) float lds[L_END];
  unsigned* ldsu = reinterpret_cast<unsigned*>(lds);
  const int t = threadIdx.x, lane = t & 63;
  const int w = __builtin_amdgcn_readfirstlane(t >> 6);
  const int g = blockIdx.x;
  const int xcd = g & 7, r = g >> 3, hsel = (r >> 3) & 1, b = r >> 4, s = r & 7;
  const int h = xcd + 8 * hsel;
  const int i16 = hsel * 8 + s;
  const int pidx = h * 8 + s;
  const bool has_ffn = i16 < 14;
  const int slice = xcd * 14 + (has_ffn ? i16 : 13);
  const int zrow = has_ffn ? slice : (112 + xcd * 2 + (i16 - 14));
  const bool is_cons = i16 >= 9;
  const int ci = xcd * 7 + (is_cons ? (i16 - 9) : 0);
  const int half = lane >> 5, l32 = lane & 31;
  const int l56 = lane < 56 ? lane : lane - 8;

  const __amdgpu_buffer_rsrc_t rs = MKRS(ws, WS_END);
  const u32x4 rws = mkdesc(ws, WS_END);
  const size_t kvoff = ((size_t)(b * H_ + h) * SK + s * KEYS_WG + w * 8) * DH;
  Ctx cx;
  cx.rk = mkdesc(kc + kvoff, 524288 - w * 4096);
  cx.rv = mkdesc(vc + kvoff, 524288 - w * 4096);
  cx.ro = mkdesc(wo + (size_t)(h * 256 + w * 32) * D_, 32 * 896);
  cx.lo16 = lane * 16;
  cx.wvo = l56 * 16;

  STAMP(0);
  u32x4 R[WK];
#pragma unroll
  for (int i = 0; i < KA; ++i) asm volatile("" : "=v"(R[i]));
  u32x4 ep;
  asm volatile("s_nop 4");
  ld_sc1(ep, WS_EPOCH + b * 256, rws);
  u32x4 cpf;
  asm volatile("" : "=v"(cpf));
  u32x4 xv, Fq[8], Fg[8], Fd[4];
  const bool worker = (w >= 1 && w <= 4);
  const int tw = t - 64;
  const int mh = t & 3, j0 = (tw >> 2) & 63;
  const int j3 = (j0 < 32) ? (j0 + 192) : j0;
  if (w == 0) {
    if (lane == 0) ldsu[L_CNT] = 0u;
    issue_range<0>(R, cx, std::make_integer_sequence<int, KA0>{});
  } else if (worker) {
    ld_g(xv, x + b * D_ + l56 * 8);
    const u16* qb = wq + h * 256 + s * 32 + mh * 8;
#pragma unroll
    for (int i = 0; i < 3; ++i) {
      ld_g(Fq[2 * i], qb + (size_t)(2 * (j0 + 64 * i)) * 4096);
      ld_g(Fq[2 * i + 1], qb + (size_t)(2 * (j0 + 64 * i) + 1) * 4096);
    }
    ld_g(Fq[6], qb + (size_t)(2 * j3) * 4096);
    ld_g(Fq[7], qb + (size_t)(2 * j3 + 1) * 4096);
    const u16* base = ((mh & 2) ? wu : wg) + slice * 16 + (mh & 1) * 8;
#pragma unroll
    for (int i = 0; i < 3; ++i) {
      ld_g(Fg[2 * i], base + (size_t)(2 * (j0 + 64 * i)) * F_);
      ld_g(Fg[2 * i + 1], base + (size_t)(2 * (j0 + 64 * i) + 1) * F_);
    }
    ld_g(Fg[6], base + (size_t)(2 * j3) * F_);
    ld_g(Fg[7], base + (size_t)(2 * j3 + 1) * F_);
    const u16* pd = wd + (size_t)(slice * 16 + 4 * (w - 1)) * D_ + l56 * 8;
#pragma unroll
    for (int i = 0; i < 4; ++i) ld_g(Fd[i], pd + i * D_);
  }
  bar();
  if (w == 0) {
    __builtin_amdgcn_s_setprio(3);
    issue_range<KA0>(R, cx, std::make_integer_sequence<int, KA - KA0>{});
    waitv<KA>(ep);
    const unsigned tl0 = __builtin_amdgcn_readfirstlane(ep[0]) + 1u;
    {
      unsigned spins = 0;
      while (__hip_atomic_load(ldsu + L_CNT, __ATOMIC_RELAXED, __HIP_MEMORY_SCOPE_WORKGROUP) != 4u && ++spins < (1u << 22)) __builtin_amdgcn_s_sleep(1);
      asm volatile("" ::: "memory");
    }
    STAMP(2);
    if (lane < 8) {
      f32x4 a = {0.f, 0.f, 0.f, 0.f};
#pragma unroll
      for (int ww = 0; ww < 4; ++ww) a += *reinterpret_cast<const f32x4*>(lds + L_FRED + ww * 64 + 32 + lane * 4);
      u32x4 gr = {pk_bf16(a[0], a[1]), tl0, pk_bf16(a[2], a[3]), tl0};
      __builtin_amdgcn_raw_buffer_store_b128(gr, rs, WS_Q + (b * H_ + h) * 1024 + s * 128 + lane * 16, 0, 16);
    }
    const int q_off = WS_Q + (b * H_ + h) * 1024 + lane * 16;
    u32x4 qg;
    unsigned spins = 0;
    do {
      ld_sc1(qg, q_off, rws);
      waitv<0>(qg);
    } while (!__all(qg[1] == tl0 && qg[3] == tl0) && ++spins < (1u << 18));
    *reinterpret_cast<u32x2*>(ldsu + L_Q + lane * 2) = u32x2{qg[0], qg[2]};
    __builtin_amdgcn_s_setprio(0);
    STAMP(3);
  } else if (worker) {
    waitv<20>(xv);
    {
      float xf[8];
#pragma unroll
      for (int k = 0; k < 4; ++k) { xf[2 * k] = bflo(xv[k]); xf[2 * k + 1] = bfhi(xv[k]); }
      float sm = 0.f;
#pragma unroll
      for (int k = 0; k < 8; ++k) sm += xf[k];
      sm = lane < 56 ? sm : 0.f;
      sm = wave_sum(sm);
      const float mexact = sm / (float)D_;
      const float m = rbf(mexact);
      float vs = 0.f;
#pragma unroll
      for (int k = 0; k < 8; ++k) { float d = xf[k] - mexact; vs += d * d; }
      vs = lane < 56 ? vs : 0.f;
      vs = wave_sum(vs);
      const float var = rbf(vs / (float)D_);
      const float den = rbf(sqrtf(rbf(var + 1e-5f)));
      u32x4 xn;
#pragma unroll
      for (int k = 0; k < 4; ++k) xn[k] = pk_bf16(rbf(xf[2 * k] - m) / den, rbf(xf[2 * k + 1] - m) / den);
      if (lane < 56) {
        *reinterpret_cast<u32x4*>(ldsu + L_XN + w * 256 + lane * 4) = xn;
        if (w == 1) *reinterpret_cast<u32x4*>(ldsu + L_X + lane * 4) = xv;
      }
    }
    STAMP(1);
    unsigned xp[4];
#pragma unroll
    for (int i = 0; i < 3; ++i) xp[i] = ldsu[L_XN + w * 256 + j0 + 64 * i];
    xp[3] = ldsu[L_XN + w * 256 + j3];
    xp[3] = (j0 < 32) ? xp[3] : 0u;
    const int fo = L_FRED + (w - 1) * 64 + mh * 8 + (lane >> 5) * 4 + ((lane >> 4) & 1) * 2;
    asm volatile("s_waitcnt vmcnt(12)" : "+v"(Fq[0]), "+v"(Fq[1]), "+v"(Fq[2]), "+v"(Fq[3]), "+v"(Fq[4]), "+v"(Fq[5]), "+v"(Fq[6]), "+v"(Fq[7]));
    const f32x2 q2 = gemv8(Fq, xp);
    if ((lane & 12) == 0) *reinterpret_cast<f32x2*>(lds + fo + 32) = q2;
    asm volatile("s_waitcnt vmcnt(4)" : "+v"(Fg[0]), "+v"(Fg[1]), "+v"(Fg[2]), "+v"(Fg[3]), "+v"(Fg[4]), "+v"(Fg[5]), "+v"(Fg[6]), "+v"(Fg[7]));
    const f32x2 f2 = gemv8(Fg, xp);
    if ((lane & 12) == 0) *reinterpret_cast<f32x2*>(lds + fo) = f2;
    asm volatile("s_waitcnt lgkmcnt(0)" ::: "memory");
    if (lane == 0) __hip_atomic_fetch_add(ldsu + L_CNT, 1u, __ATOMIC_RELAXED, __HIP_MEMORY_SCOPE_WORKGROUP);
    STAMP(2);
    {
      unsigned spins = 0;
      while (__hip_atomic_load(ldsu + L_CNT, __ATOMIC_RELAXED, __HIP_MEMORY_SCOPE_WORKGROUP) != 4u && ++spins < (1u << 22)) __builtin_amdgcn_s_sleep(1);
      asm volatile("" ::: "memory");
    }
    f32x4 gg = {0.f, 0.f, 0.f, 0.f}, uu = {0.f, 0.f, 0.f, 0.f};
#pragma unroll
    for (int ww = 0; ww < 4; ++ww) {
      gg += *reinterpret_cast<const f32x4*>(lds + L_FRED + ww * 64 + 4 * (w - 1));
      uu += *reinterpret_cast<const f32x4*>(lds + L_FRED + ww * 64 + 16 + 4 * (w - 1));
    }
    float hm[4];
#pragma unroll
    for (int k = 0; k < 4; ++k) {
      const float gv = rbf(gg[k]), uv = rbf(uu[k]);
      const float sg = rbf(gv / (1.f + __expf(-gv)));
      hm[k] = sg * uv;
    }
    const unsigned hp0 = pk_bf16(hm[0], hm[1]), hp1 = pk_bf16(hm[2], hm[3]);
    asm volatile("s_waitcnt vmcnt(0)" : "+v"(Fd[0]), "+v"(Fd[1]), "+v"(Fd[2]), "+v"(Fd[3]));
    f32x4 z0, z1;
#pragma unroll
    for (int k = 0; k < 2; ++k) {
      z0[2 * k] = dot2(perm_lo(Fd[2][k], Fd[3][k]), hp1, dot2(perm_lo(Fd[0][k], Fd[1][k]), hp0, 0.f));
      z0[2 * k + 1] = dot2(perm_hi(Fd[2][k], Fd[3][k]), hp1, dot2(perm_hi(Fd[0][k], Fd[1][k]), hp0, 0.f));
      z1[2 * k] = dot2(perm_lo(Fd[2][k + 2], Fd[3][k + 2]), hp1, dot2(perm_lo(Fd[0][k + 2], Fd[1][k + 2]), hp0, 0.f));
      z1[2 * k + 1] = dot2(perm_hi(Fd[2][k + 2], Fd[3][k + 2]), hp1, dot2(perm_hi(Fd[0][k + 2], Fd[1][k + 2]), hp0, 0.f));
    }
    if (lane < 56) {
      *reinterpret_cast<f32x4*>(lds + L_YRED + (w - 1) * 448 + lane * 8) = z0;
      *reinterpret_cast<f32x4*>(lds + L_YRED + (w - 1) * 448 + lane * 8 + 4) = z1;
    }
    STAMP(3);
  }
  if (w != 0) issue_range<0>(R, cx, std::make_integer_sequence<int, KA>{});
  asm volatile("" ::: "memory");
  if (w == 0) bar();
  issue_range<KA>(R, cx, std::make_integer_sequence<int, WK - KA>{});
  if (w != 0) bar();
  waitv<WK - KA>(ep);
  PIN1(cpf);
  const unsigned long long e64 = (((unsigned long long)ep[1] << 32) | ep[0]) + 1ull;
  const unsigned tlo = __builtin_amdgcn_readfirstlane((unsigned)e64);
  const unsigned thi_full = __builtin_amdgcn_readfirstlane((unsigned)(e64 >> 32));
  const unsigned thi = thi_full & 0xffu;
  const unsigned g0tag = tlo << 24, g1tag = (tlo >> 8) | (thi << 24);
  const u32x4 q4 = *reinterpret_cast<const u32x4*>(ldsu + L_Q + l32 * 4);
  if (t < 224) {
    f32x2 zz = {0.f, 0.f};
#pragma unroll
    for (int ww = 0; ww < 4; ++ww) zz += *reinterpret_cast<const f32x2*>(lds + L_YRED + ww * 448 + 2 * t);
    const unsigned d0 = (__float_as_uint(zz[0]) + 0x80u) >> 8, d1 = (__float_as_uint(zz[1]) + 0x80u) >> 8;
    u32x4 gr = {d0 | g0tag, g1tag, d1 | g0tag, g1tag};
    __builtin_amdgcn_raw_buffer_store_b128(gr, rs, WS_Z + ((b * 56 + (t >> 2)) * 128 + zrow) * 64 + (t & 3) * 16, 0, 16);
  }
  STAMP(4);

  KState ks;
  kphase(R, cx, q4, ks, lane, std::make_integer_sequence<int, 64>{});
  STAMP(5);
  const float mw = wave_max(fmaxf(ks.sc[0], ks.sc[1]));
  {
    const float p0 = __expf(ks.sc[0] - mw), p1 = __expf(ks.sc[1] - mw);
    const float lw = wave_sum(p0 + p1);
    const int li = ((lane >> 4) & 1) | ((lane >> 2) & 2) | (lane & 4) | ((lane << 2) & 8) | ((lane << 4) & 16);
    lds[L_P + w * 128 + half * 64 + li] = p0;
    lds[L_P + w * 128 + half * 64 + 32 + li] = p1;
    if (lane == 0) {
      u32x4 gr = {__float_as_uint(mw), tlo, __float_as_uint(lw), tlo};
      __builtin_amdgcn_raw_buffer_store_b128(gr, rs, WS_ML + ((b * H_ + h) * 64 + s * 8 + w) * 16, 0, 16);
    }
  }

  VState vs;
#pragma unroll
  for (int k = 0; k < 8; ++k) vs.acc[k] = 0.f;
  const int ml_off = WS_ML + ((b * H_ + h) * 64 + lane) * 16;
  const int z_off = WS_Z + ((b * 56 + ci) * 128) * 64 + t * 16;
  vphase(R, cx, vs, lds + L_P + w * 128 + half * 64, rws, ml_off, z_off, std::make_integer_sequence<int, 64>{});
  asm volatile("s_waitcnt vmcnt(0)" : "+v"(vs.mlg), "+v"(vs.zg));
  STAMP(6);
  {
    unsigned spins = 0;
    while (!__all(vs.mlg[1] == tlo && vs.mlg[3] == tlo) && ++spins < (1u << 18)) {
      __builtin_amdgcn_s_sleep(1);
      ld_sc1(vs.mlg, ml_off, rws);
      waitv<0>(vs.mlg);
    }
  }
  {
    const float mi = __uint_as_float(vs.mlg[0]), li = __uint_as_float(vs.mlg[2]);
    const float M = wave_max(mi);
    const float L = wave_sum(li * __expf(mi - M));
    const float f = __expf(mw - M) / L;
    f32x4 oh;
#pragma unroll
    for (int k = 0; k < 4; ++k) oh[k] = sw32(vs.acc[k], vs.acc[k + 4]) * f;
    *reinterpret_cast<f32x4*>(lds + L_ORED + w * 256 + l32 * 8 + half * 4) = oh;
  }
  STAMP(7);
  bar();
  STAMP(8);
  {
    float ov = 0.f;
#pragma unroll
    for (int ww = 0; ww < 8; ++ww) ov += lds[L_ORED + ww * 256 + w * 32 + l32];
    const float on = dpp<XOR1>(ov);
    const unsigned opk = pk_bf16(ov, on);
    float ya[8];
#pragma unroll
    for (int k = 0; k < 8; ++k) ya[k] = 0.f;
    ophase(R, opk, ya, std::make_integer_sequence<int, 16>{});
    if (lane < 56) {
      *reinterpret_cast<f32x4*>(lds + L_YRED + w * 448 + lane * 8) = f32x4{ya[0], ya[1], ya[2], ya[3]};
      *reinterpret_cast<f32x4*>(lds + L_YRED + w * 448 + lane * 8 + 4) = f32x4{ya[4], ya[5], ya[6], ya[7]};
    }
  }
  bar();
  if (t < 224) {
    f32x2 yy = {0.f, 0.f};
#pragma unroll
    for (int ww = 0; ww < 8; ++ww) yy += *reinterpret_cast<const f32x2*>(lds + L_YRED + ww * 448 + 2 * t);
    const unsigned d0 = (__float_as_uint(yy[0]) + 0x80u) >> 8, d1 = (__float_as_uint(yy[1]) + 0x80u) >> 8;
    u32x4 gr = {d0 | g0tag, g1tag, d1 | g0tag, g1tag};
    __builtin_amdgcn_raw_buffer_store_b128(gr, rs, WS_Y + ((b * 56 + (t >> 2)) * 128 + pidx) * 64 + (t & 3) * 16, 0, 16);
  }
  STAMP(9);
  if (!is_cons) return;

  {
    const int y_off = WS_Y + ((b * 56 + ci) * 128) * 64 + t * 16;
    u32x4 yg;
    ld_sc1(yg, y_off, rws);
    const bool zlive = t < 448;
    u32x4 zg = vs.zg;
    unsigned spins = 0;
    while (!__all(!zlive || ((zg[0] & 0xff000000u) == g0tag && zg[1] == g1tag && (zg[2] & 0xff000000u) == g0tag && zg[3] == g1tag)) && ++spins < (1u << 18)) {
      __builtin_amdgcn_s_sleep(1);
      ld_sc1(zg, z_off, rws);
      waitv<0>(zg);
    }
    float z0 = zlive ? __uint_as_float(zg[0] << 8) : 0.f, z1 = zlive ? __uint_as_float(zg[2] << 8) : 0.f;
    z0 += dpp<ROR4>(z0); z0 += dpp<XOR8>(z0); z0 = x16_sum(z0); z0 = x32_sum(z0);
    z1 += dpp<ROR4>(z1); z1 += dpp<XOR8>(z1); z1 = x16_sum(z1); z1 = x32_sum(z1);
    PIN1(z0); PIN1(z1);
    waitv<0>(yg);
    spins = 0;
    while (!__all((yg[0] & 0xff000000u) == g0tag && yg[1] == g1tag && (yg[2] & 0xff000000u) == g0tag && yg[3] == g1tag) && ++spins < (1u << 18)) {
      ld_sc1(yg, y_off, rws);
      waitv<0>(yg);
    }
    float y0 = __uint_as_float(yg[0] << 8), y1 = __uint_as_float(yg[2] << 8);
    y0 += dpp<ROR4>(y0); y0 += dpp<XOR8>(y0); y0 = x16_sum(y0); y0 = x32_sum(y0);
    y1 += dpp<ROR4>(y1); y1 += dpp<XOR8>(y1); y1 = x16_sum(y1); y1 = x32_sum(y1);
    if (lane < 4) {
      *reinterpret_cast<f32x2*>(lds + L_C + w * 16 + lane * 2) = f32x2{y0, y1};
      *reinterpret_cast<f32x2*>(lds + L_C + w * 16 + 8 + lane * 2) = f32x2{z0, z1};
    }
    STAMP(10);
    bar();
    if (t < 8) {
      float a = 0.f, f = 0.f;
#pragma unroll
      for (int ww = 0; ww < 8; ++ww) { a += lds[L_C + ww * 16 + t]; f += lds[L_C + ww * 16 + 8 + t]; }
      const unsigned xo = reinterpret_cast<const u16*>(ldsu + L_X)[ci * 8 + t];
      const float xr = __uint_as_float(xo << 16);
      const float res = rbf(xr + rbf(a)) + rbf(f);
      out[b * D_ + ci * 8 + t] = __builtin_bit_cast(u16, (__bf16)res);
    }
    STAMP(11);
    if (ci == 0 && t == 0) {
      u32x2 ne = {tlo, thi_full};
      __builtin_amdgcn_raw_buffer_store_b64(ne, rs, WS_EPOCH + b * 256, 0, 16);
    }
  }
}

extern "C" void kernel_launch(void* const* d_in, const int* in_sizes, int n_in,
                              void* d_out, int out_size, void* d_ws, size_t ws_size,
                              hipStream_t stream) {
  const u16* x = (const u16*)d_in[0];
  const u16* k = (const u16*)d_in[1];
  const u16* v = (const u16*)d_in[2];
  const u16* wq = (const u16*)d_in[3];
  const u16* wo = (const u16*)d_in[4];
  const u16* wg = (const u16*)d_in[6];
  const u16* wu = (const u16*)d_in[7];
  const u16* wd = (const u16*)d_in[8];
  fused_kernel<<<NWG, NTHR, 0, stream>>>(x, k, v, wq, wo, wg, wu, wd, (u16*)d_out, (unsigned char*)d_ws);
}
```

```cpp
#include <hip/hip_runtime.h>
#include <hip/hip_bf16.h>
#include <stdint.h>
#include <utility>
#define NOTOUCH 1
#define W0LATE 1

typedef unsigned short u16;
typedef unsigned u32x4 __attribute__((ext_vector_type(4)));
typedef unsigned u32x2 __attribute__((ext_vector_type(2)));
typedef float f32x4 __attribute__((ext_vector_type(4)));
typedef float f32x2 __attribute__((ext_vector_type(2)));
typedef __bf16 bf16x2 __attribute__((ext_vector_type(2)));

constexpr int D_ = 448, H_ = 16, SK = 8192, DH = 256, F_ = 1792;
constexpr int NWG = 256, NTHR = 512;
constexpr int NSPLIT = 8;
constexpr int KEYS_WG = SK / NSPLIT;
#define WKK 48
constexpr int WK = WKK;
#define KAA 16
#define KA0 16
constexpr int KA = KAA;
#define VAA 48
constexpr int VA = VAA;
#define BARN 8
#define CODEPF 23552
#define WOP 3
#define WOS 0
constexpr int NISS = 160;
constexpr int nwo_before(int c) { return c <= WOS ? 0 : ((c - WOS) / WOP < 32 ? (c - WOS) / WOP : 32); }
constexpr bool is_wo_step(int c) { return c >= WOS && ((c - WOS) % WOP) == WOP - 1 && (c - WOS) / WOP < 32; }
constexpr int kv_of_step(int c) { return WK + c - nwo_before(c); }
constexpr int idx_of_kv(int m) {
  if (m < WK) return m;
  for (int c = 0; c < NISS; ++c) if (!is_wo_step(c) && kv_of_step(c) == m) return WK + c;
  return -1;
}
constexpr int idx_of_wo(int k) { return WK + WOS + k * WOP + WOP - 1; }
constexpr int idx_of_step(int c) { return idx_of_kv(c); }
constexpr int slot_of(int n) { return n < WK ? n : slot_of(idx_of_step(n - WK)); }
constexpr int wait_of_step(int c) { return ((WK + c < NISS) ? WK + c : NISS) - idx_of_step(c) - 1; }
static_assert(idx_of_kv(127) < NISS && idx_of_kv(127) > 0 && idx_of_wo(31) < NISS, "plan");
static_assert(idx_of_kv(112) > idx_of_wo(1) && idx_of_kv(127) > idx_of_wo(31) && idx_of_kv(120) > idx_of_wo(17), "wo landed before its pre-permute");

constexpr int WS_EPOCH = 0;
constexpr int WS_ML = 4096;
constexpr int WS_Q = WS_ML + 2 * 16 * 64 * 16;
constexpr int WS_Y = WS_Q + 2 * 16 * 4096;
constexpr int WS_Z = WS_Y + 2 * 56 * 128 * 64;
constexpr int WS_END = WS_Z + 2 * 56 * 128 * 64;

constexpr int L_XN = 0;
constexpr int L_ORED = L_XN + 8 * 256;
constexpr int L_FRED = L_ORED + 8 * 256;
constexpr int L_YRED = L_FRED + 8 * 64;
constexpr int L_P = L_YRED + 8 * 448;
constexpr int L_C = L_P + 8 * 128;
constexpr int L_X = L_C + 8 * 16;
constexpr int L_Q = L_X + 256;
constexpr int L_CNT = L_Q + 128;
constexpr int L_QP = L_CNT + 4;
constexpr int L_END = L_QP + 256;

__device__ __forceinline__ float dot2(unsigned a, unsigned b, float c) {
  return __builtin_amdgcn_fdot2_f32_bf16(__builtin_bit_cast(bf16x2, a), __builtin_bit_cast(bf16x2, b), c, false);
}
__device__ __forceinline__ float bflo(unsigned v) { return __uint_as_float(v << 16); }
__device__ __forceinline__ float bfhi(unsigned v) { return __uint_as_float(v & 0xffff0000u); }
__device__ __forceinline__ unsigned pk_bf16(float lo, float hi) {
  bf16x2 r; r[0] = (__bf16)lo; r[1] = (__bf16)hi; return __builtin_bit_cast(unsigned, r);
}
__device__ __forceinline__ float rbf(float x) { return (float)(__bf16)x; }
__device__ __forceinline__ unsigned perm_lo(unsigned a, unsigned b) { return __builtin_amdgcn_perm(b, a, 0x05040100u); }
__device__ __forceinline__ unsigned perm_hi(unsigned a, unsigned b) { return __builtin_amdgcn_perm(b, a, 0x07060302u); }
template <int CTRL> __device__ __forceinline__ float dpp(float x) {
  return __builtin_bit_cast(float, __builtin_amdgcn_mov_dpp(__builtin_bit_cast(int, x), CTRL, 0xf, 0xf, true));
}
constexpr int XOR1 = 0xB1, XOR2 = 0x4E, XOR7 = 0x141, XOR8 = 0x128, ROR4 = 0x124;
__device__ __forceinline__ float sw16(float a, float b) {
  auto s = __builtin_amdgcn_permlane16_swap(__float_as_uint(a), __float_as_uint(b), false, false);
  return __uint_as_float(s[0]) + __uint_as_float(s[1]);
}
__device__ __forceinline__ float sw32(float a, float b) {
  auto s = __builtin_amdgcn_permlane32_swap(__float_as_uint(a), __float_as_uint(b), false, false);
  return __uint_as_float(s[0]) + __uint_as_float(s[1]);
}
__device__ __forceinline__ float x16_sum(float x) { return sw16(x, x); }
__device__ __forceinline__ float x32_sum(float x) { return sw32(x, x); }
__device__ __forceinline__ float x16_max(float x) {
  auto s = __builtin_amdgcn_permlane16_swap(__float_as_uint(x), __float_as_uint(x), false, false);
  return fmaxf(__uint_as_float(s[0]), __uint_as_float(s[1]));
}
__device__ __forceinline__ float x32_max(float x) {
  auto s = __builtin_amdgcn_permlane32_swap(__float_as_uint(x), __float_as_uint(x), false, false);
  return fmaxf(__uint_as_float(s[0]), __uint_as_float(s[1]));
}
__device__ __forceinline__ float wave_sum(float x) {
  x += dpp<XOR1>(x); x += dpp<XOR2>(x); x += dpp<XOR7>(x); x += dpp<XOR8>(x);
  x = x16_sum(x); x = x32_sum(x); return x;
}
__device__ __forceinline__ float wave_max(float x) {
  x = fmaxf(x, dpp<XOR1>(x)); x = fmaxf(x, dpp<XOR2>(x)); x = fmaxf(x, dpp<XOR7>(x)); x = fmaxf(x, dpp<XOR8>(x));
  x = x16_max(x); x = x32_max(x); return x;
}
template <int CTRL, int BIT> __device__ __forceinline__ float trs(float lo, float hi, int lane) {
  const float t1 = lo + dpp<CTRL>(lo), t2 = hi + dpp<CTRL>(hi);
  return (lane & BIT) ? t2 : t1;
}
__device__ __forceinline__ void bar() { asm volatile("s_waitcnt lgkmcnt(0)\n\ts_barrier" ::: "memory"); }
#define SB() __builtin_amdgcn_sched_barrier(0)
#define PIN8(a_) asm volatile("" : "+v"(a_[0]), "+v"(a_[1]), "+v"(a_[2]), "+v"(a_[3]), "+v"(a_[4]), "+v"(a_[5]), "+v"(a_[6]), "+v"(a_[7]))
#define PIN1(a_) asm volatile("" : "+v"(a_))
#define MKRS(ptr_, bytes_) __builtin_amdgcn_make_buffer_rsrc((void*)(ptr_), 0, (bytes_), 0x00020000)
#define STAMP(i_)

__device__ __forceinline__ u32x4 mkdesc(const void* p, unsigned bytes) {
  const unsigned long long a = (unsigned long long)p;
  u32x4 d;
  d[0] = __builtin_amdgcn_readfirstlane((unsigned)a);
  d[1] = __builtin_amdgcn_readfirstlane((unsigned)(a >> 32) & 0xffffu);
  d[2] = __builtin_amdgcn_readfirstlane(bytes);
  d[3] = 0x00020000u;
  return d;
}
#define K_POL " nt"
#define V_POL " nt"
template <int OFF> __device__ __forceinline__ void ld_nt_new(u32x4& d, int voff, const u32x4& rsrc, int soff) {
  asm volatile("buffer_load_dwordx4 %0, %1, %2, %3 offen offset:%4" K_POL : "=v"(d) : "v"(voff), "s"(rsrc), "s"(soff), "i"(OFF));
}
template <int OFF> __device__ __forceinline__ void ld_nt_re(u32x4& d, int voff, const u32x4& rsrc, int soff) {
  asm volatile("buffer_load_dwordx4 %0, %1, %2, %3 offen offset:%4" K_POL : "+v"(d) : "v"(voff), "s"(rsrc), "s"(soff), "i"(OFF));
}
template <int OFF> __device__ __forceinline__ void ld_v_re(u32x4& d, int voff, const u32x4& rsrc, int soff) {
  asm volatile("buffer_load_dwordx4 %0, %1, %2, %3 offen offset:%4" V_POL : "+v"(d) : "v"(voff), "s"(rsrc), "s"(soff), "i"(OFF));
}
#define WO_POL ""
#define W_POL ""
#define C_POL ""
template <int OFF> __device__ __forceinline__ void ld_pl_re(u32x4& d, int voff, const u32x4& rsrc, int soff) {
  asm volatile("buffer_load_dwordx4 %0, %1, %2, %3 offen offset:%4" WO_POL : "+v"(d) : "v"(voff), "s"(rsrc), "s"(soff), "i"(OFF));
}
__device__ __forceinline__ void ld_g(u32x4& d, const void* p) {
  asm volatile("global_load_dwordx4 %0, %1, off" W_POL : "=v"(d) : "v"(p));
}
__device__ __forceinline__ void ld_b(u32x4& d, int voff, const u32x4& rsrc, int soff) {
  asm volatile("buffer_load_dwordx4 %0, %1, %2, %3 offen" C_POL : "=v"(d) : "v"(voff), "s"(rsrc), "s"(soff));
}
__device__ __forceinline__ void ld_sc1(u32x4& d, int voff, const u32x4& rsrc) {
  asm volatile("buffer_load_dwordx4 %0, %1, %2, 0 offen sc1" : "=v"(d) : "v"(voff), "s"(rsrc) : "memory");
}
template <int OFF> __device__ __forceinline__ void ld_w(u32x4& d, int voff, const u32x4& rsrc) {
  asm volatile("buffer_load_dwordx4 %0, %1, %2, 0 offen offset:%3" W_POL : "=v"(d) : "v"(voff), "s"(rsrc), "i"(OFF));
}
__device__ __forceinline__ void ld_sc1_re(u32x4& d, int voff, const u32x4& rsrc) {
  asm volatile("buffer_load_dwordx4 %0, %1, %2, 0 offen sc1\n\ts_waitcnt vmcnt(0)" : "+v"(d) : "v"(voff), "s"(rsrc) : "memory");
}
template <int N> __device__ __forceinline__ void waitv(u32x4& d) { asm volatile("s_waitcnt vmcnt(%1)" : "+v"(d) : "i"(N)); }

struct Ctx {
  u32x4 rk, rv, ro;
  int lo16, wvo;
};
#define YSLEEP 2
#define FFNC 56
#define FSTEP 60
#define PACE1 63
#define PACE2 63
template <int n> __device__ __forceinline__ void issue(u32x4 (&R)[WK], const Ctx& c) {
  if constexpr (n < NISS) {
    constexpr int sl = slot_of(n);
    if constexpr (n < WK) { if constexpr (PACE1 < 63 && n > PACE1) asm volatile("s_waitcnt vmcnt(%0)" :: "i"(PACE1)); }
    else { if constexpr (PACE2 < 63) asm volatile("s_waitcnt vmcnt(%0)" :: "i"(PACE2)); }
    if constexpr (n < WK) {
      if constexpr (n >= KA) ld_nt_new<(n & 3) * 1024>(R[sl], c.lo16, c.rk, (n >> 2) * 32768);
      else ld_nt_re<(n & 3) * 1024>(R[sl], c.lo16, c.rk, (n >> 2) * 32768);
    } else if constexpr (is_wo_step(n - WK)) {
      constexpr int k = nwo_before(n - WK);
      ld_pl_re<(k & 3) * 896>(R[sl], c.wvo, c.ro, (k >> 2) * 3584);
    } else {
      constexpr int m = kv_of_step(n - WK);
      if constexpr (m < 64) ld_nt_re<(m & 3) * 1024>(R[sl], c.lo16, c.rk, (m >> 2) * 32768);
      else ld_v_re<((m - 64) & 3) * 1024>(R[sl], c.lo16, c.rv, ((m - 64) >> 2) * 32768);
    }
  }
}
template <int A, int... I> __device__ __forceinline__ void issue_range(u32x4 (&R)[WK], const Ctx& c, std::integer_sequence<int, I...>) {
  (issue<A + I>(R, c), ...);
}

struct KState { float t0, t1, t2, t3, t4; float sc[2]; };
template <int c> __device__ __forceinline__ void kstep(u32x4 (&R)[WK], const Ctx& cx, const u32x4& q4, KState& st, int lane) {
  constexpr int sl = slot_of(idx_of_step(c));
  waitv<wait_of_step(c)>(R[sl]);
  float d = dot2(R[sl][0], q4[0], 0.f);
  d = dot2(R[sl][1], q4[1], d);
  d = dot2(R[sl][2], q4[2], d);
  d = dot2(R[sl][3], q4[3], d);
  PIN1(d);
  issue<WK + c>(R, cx);
  if constexpr (BARN && (c % BARN) == BARN - 1) __builtin_amdgcn_s_barrier();
  constexpr int i = c & 31;
  if constexpr ((i & 1) == 0) { st.t0 = d; return; }
  else {
    d = sw16(st.t0, d);
    if constexpr ((i & 2) == 0) { st.t1 = d; return; }
    else {
      d = trs<XOR8, 8>(st.t1, d, lane);
      if constexpr ((i & 4) == 0) { st.t2 = d; return; }
      else {
        d = trs<XOR7, 4>(st.t2, d, lane);
        if constexpr ((i & 8) == 0) { st.t3 = d; return; }
        else {
          d = trs<XOR2, 2>(st.t3, d, lane);
          if constexpr ((i & 16) == 0) { st.t4 = d; return; }
          else {
            d = trs<XOR1, 1>(st.t4, d, lane);
            st.sc[c >> 5] = rbf(d) * 0.0625f;
          }
        }
      }
    }
  }
}
template <int A, int... I> __device__ __forceinline__ void kphase(u32x4 (&R)[WK], const Ctx& cx, const u32x4& q4, KState& st, int lane, std::integer_sequence<int, I...>) {
  (kstep<A + I>(R, cx, q4, st, lane), ...);
}

struct VState { float acc[8]; f32x4 p4; u32x4 mlg, zg; };
constexpr int GATHER_STEP = 40;
template <int j> __device__ __forceinline__ void vstep(u32x4 (&R)[WK], const Ctx& cx, VState& st, const float* pl, const u32x4& rws, int ml_off, int z_off) {
  constexpr int c = 64 + j;
  constexpr int sl = slot_of(idx_of_step(c));
  if constexpr ((j & 3) == 0) st.p4 = *reinterpret_cast<const f32x4*>(pl + j);
  waitv<wait_of_step(c)>(R[sl]);
  const float p = st.p4[j & 3];
#pragma unroll
  for (int k = 0; k < 4; ++k) {
    st.acc[2 * k] = fmaf(p, bflo(R[sl][k]), st.acc[2 * k]);
    st.acc[2 * k + 1] = fmaf(p, bfhi(R[sl][k]), st.acc[2 * k + 1]);
  }
  PIN8(st.acc);
  issue<WK + c>(R, cx);
  if constexpr (j == GATHER_STEP) { ld_sc1(st.mlg, ml_off, rws); ld_sc1(st.zg, z_off, rws); }
  if constexpr (j >= 48) {
    constexpr int i = j - 48;
    static_assert(idx_of_kv(64 + j) > idx_of_wo(2 * i + 1), "wo pair landed");
    constexpr int sa = slot_of(idx_of_wo(2 * i)), sb = slot_of(idx_of_wo(2 * i + 1));
    PIN1(R[sa]); PIN1(R[sb]);
#pragma unroll
    for (int k = 0; k < 4; ++k) {
      const unsigned a = R[sa][k], e = R[sb][k];
      R[sa][k] = perm_lo(a, e); R[sb][k] = perm_hi(a, e);
    }
    PIN1(R[sa]); PIN1(R[sb]);
  }
  if constexpr (BARN && (j % BARN) == BARN - 1 && j < 48) __builtin_amdgcn_s_barrier();
}
template <int A, int... I> __device__ __forceinline__ void vphase(u32x4 (&R)[WK], const Ctx& cx, VState& st, const float* pl, const u32x4& rws, int ml_off, int z_off, std::integer_sequence<int, I...>) {
  (vstep<A + I>(R, cx, st, pl, rws, ml_off, z_off), ...);
}
template <int i> __device__ __forceinline__ void ostep(u32x4 (&R)[WK], unsigned opk, float (&ya)[8]) {
  constexpr int sa = slot_of(idx_of_wo(2 * i)), sb = slot_of(idx_of_wo(2 * i + 1));
  const unsigned so = __builtin_amdgcn_readlane(opk, 2 * i);
#pragma unroll
  for (int k = 0; k < 4; ++k) {
    ya[2 * k] = dot2(R[sa][k], so, ya[2 * k]);
    ya[2 * k + 1] = dot2(R[sb][k], so, ya[2 * k + 1]);
  }
}
template <int... I> __device__ __forceinline__ void ophase(u32x4 (&R)[WK], unsigned opk, float (&ya)[8], std::integer_sequence<int, I...>) {
  (ostep<I>(R, opk, ya), ...);
}
static_assert(WK > 32 && KA <= WK, "plan");

__device__ __forceinline__ f32x2 gemv8(const u32x4 (&F)[8], const unsigned (&xp)[4]) {
  float fa[8];
#pragma unroll
  for (int k = 0; k < 8; ++k) fa[k] = 0.f;
#pragma unroll
  for (int i = 0; i < 4; ++i)
#pragma unroll
    for (int k = 0; k < 4; ++k) {
      fa[2 * k] = dot2(perm_lo(F[2 * i][k], F[2 * i + 1][k]), xp[i], fa[2 * k]);
      fa[2 * k + 1] = dot2(perm_hi(F[2 * i][k], F[2 * i + 1][k]), xp[i], fa[2 * k + 1]);
    }
  float f4[4];
  f32x2 f2;
#pragma unroll
  for (int k = 0; k < 4; ++k) f4[k] = sw32(fa[k], fa[k + 4]);
#pragma unroll
  for (int k = 0; k < 2; ++k) { float v = sw16(f4[k], f4[k + 2]); v += dpp<ROR4>(v); v += dpp<XOR8>(v); f2[k] = v; }
  return f2;
}

__global__ void __launch_bounds__(NTHR, 2)
fused_kernel(const u16* __restrict__ x, const u16* __restrict__ kc, const u16* __restrict__ vc,
             const u16* __restrict__ wq, const u16* __restrict__ wo, const u16* __restrict__ wg,
             const u16* __restrict__ wu, const u16* __restrict__ wd, u16* __restrict__ out,
             unsigned char* ws) {
  __shared__ __attribute__((aligned(16))) float lds[L_END];
  unsigned* ldsu = reinterpret_cast<unsigned*>(lds);
  const int t = threadIdx.x, lane = t & 63;
  const int w = __builtin_amdgcn_readfirstlane(t >> 6);
  const int g = blockIdx.x;
  const int xcd = g & 7, r = g >> 3, hsel = (r >> 3) & 1, b = r >> 4, s = r & 7;
  const int h = xcd + 8 * hsel;
  const int i16 = hsel * 8 + s;
  const int pidx = h * 8 + s;
  const bool has_ffn = i16 < 14;
  const int slice = xcd * 14 + (has_ffn ? i16 : 13);
  const int zrow = has_ffn ? slice : (112 + xcd * 2 + (i16 - 14));
  const bool is_cons = i16 >= 9;
  const int ci = xcd * 7 + (is_cons ? (i16 - 9) : 0);
  const int half = lane >> 5, l32 = lane & 31;
  const int l56 = lane < 56 ? lane : lane - 8;

  const __amdgpu_buffer_rsrc_t rs = MKRS(ws, WS_END);
  const u32x4 rws = mkdesc(ws, WS_END);
  const size_t kvoff = ((size_t)(b * H_ + h) * SK + s * KEYS_WG + w * 8) * DH;
  Ctx cx;
  cx.rk = mkdesc(kc + kvoff, 524288 - w * 4096);
  cx.rv = mkdesc(vc + kvoff, 524288 - w * 4096);
  cx.ro = mkdesc(wo + (size_t)(h * 256 + w * 32) * D_, 32 * 896);
  cx.lo16 = lane * 16;
  cx.wvo = l56 * 16;

  STAMP(0);
  u32x4 R[WK];
#pragma unroll
  for (int i = 0; i < KA; ++i) asm volatile("" : "=v"(R[i]));
  u32x4 ep;
  if (t == 0) ldsu[L_CNT] = 0u;
  bar();
  asm volatile("s_nop 4");
  ld_sc1(ep, WS_EPOCH + b * 256, rws);
  u32x4 cpf;
  asm volatile("" : "=v"(cpf));
  u32x4 xv, Fq[8];
  const bool worker = (w >= 1 && w <= 4);
  const int tw = t - 64;
  const int qline = xcd * 8 + (i16 >> 1), qrh = i16 & 1;
  const int j0 = (tw >> 3) & 31;
  const int j3 = (j0 < 16) ? (j0 + 96) : j0;
  if (w == 0) {
    issue_range<0>(R, cx, std::make_integer_sequence<int, KA0>{});
  } else if (worker) {
    ld_g(xv, x + b * D_ + l56 * 8);
    const u16* qb = wq + (size_t)(qrh * 224) * 4096 + qline * 64 + (lane & 7) * 8;
#define QS 4096
#pragma unroll
    for (int i = 0; i < 3; ++i) {
      ld_g(Fq[2 * i], qb + (size_t)(2 * (j0 + 32 * i)) * QS);
      ld_g(Fq[2 * i + 1], qb + (size_t)(2 * (j0 + 32 * i) + 1) * QS);
    }
    ld_g(Fq[6], qb + (size_t)(2 * j3) * QS);
    ld_g(Fq[7], qb + (size_t)(2 * j3 + 1) * QS);
  }
  asm volatile("" ::: "memory");
  STAMP(14);
  if (w == 0) {
    issue_range<KA0>(R, cx, std::make_integer_sequence<int, KA - KA0>{});
    waitv<KA>(ep);
    const unsigned tl0 = __builtin_amdgcn_readfirstlane(ep[0]) + 1u;
    {
      unsigned spins = 0;
      while (__hip_atomic_load(ldsu + L_CNT, __ATOMIC_RELAXED, __HIP_MEMORY_SCOPE_WORKGROUP) != 4u && ++spins < (1u << 22)) __builtin_amdgcn_s_sleep(1);
      asm volatile("" ::: "memory");
    }
    STAMP(2);
    __builtin_amdgcn_s_setprio(3);
    if (lane < 32) {
      f32x2 a = {0.f, 0.f};
#pragma unroll
      for (int ww = 0; ww < 4; ++ww) a += *reinterpret_cast<const f32x2*>(lds + L_QP + ww * 64 + lane * 2);
      u32x4 gr = {__float_as_uint(a[0]), tl0, __float_as_uint(a[1]), tl0};
      __builtin_amdgcn_raw_buffer_store_b128(gr, rs, WS_Q + ((b * H_ + (qline >> 2)) * 2 + qrh) * 2048 + ((qline & 3) * 64 + lane * 2) * 8, 0, 16);
    }
    const int q_off = WS_Q + (b * H_ + h) * 4096 + lane * 32;
    u32x4 qa0, qa1, qb0, qb1;
    unsigned spins = 0;
    bool okq;
    do {
      ld_sc1(qa0, q_off, rws); ld_sc1(qa1, q_off + 16, rws); ld_sc1(qb0, q_off + 2048, rws); ld_sc1(qb1, q_off + 2064, rws);
      asm volatile("s_waitcnt vmcnt(0)" : "+v"(qa0), "+v"(qa1), "+v"(qb0), "+v"(qb1));
      okq = qa0[1] == tl0 && qa0[3] == tl0 && qa1[1] == tl0 && qa1[3] == tl0 && qb0[1] == tl0 && qb0[3] == tl0 && qb1[1] == tl0 && qb1[3] == tl0;
    } while (!__all(okq) && ++spins < (1u << 18));
    *reinterpret_cast<u32x2*>(ldsu + L_Q + lane * 2) = u32x2{
        pk_bf16(__uint_as_float(qa0[0]) + __uint_as_float(qb0[0]), __uint_as_float(qa0[2]) + __uint_as_float(qb0[2])),
        pk_bf16(__uint_as_float(qa1[0]) + __uint_as_float(qb1[0]), __uint_as_float(qa1[2]) + __uint_as_float(qb1[2]))};
    __builtin_amdgcn_s_setprio(0);
    STAMP(3);
  } else if (worker) {
    waitv<8>(xv);
    {
      float xf[8];
#pragma unroll
      for (int k = 0; k < 4; ++k) { xf[2 * k] = bflo(xv[k]); xf[2 * k + 1] = bfhi(xv[k]); }
      float sm = 0.f;
#pragma unroll
      for (int k = 0; k < 8; ++k) sm += xf[k];
      sm = lane < 56 ? sm : 0.f;
      sm = wave_sum(sm);
      const float mexact = sm / (float)D_;
      const float m = rbf(mexact);
      float vs = 0.f;
#pragma unroll
      for (int k = 0; k < 8; ++k) { float d = xf[k] - mexact; vs += d * d; }
      vs = lane < 56 ? vs : 0.f;
      vs = wave_sum(vs);
      const float var = rbf(vs / (float)D_);
      const float den = rbf(sqrtf(rbf(var + 1e-5f)));
      u32x4 xn;
#pragma unroll
      for (int k = 0; k < 4; ++k) xn[k] = pk_bf16(rbf(xf[2 * k] - m) / den, rbf(xf[2 * k + 1] - m) / den);
      if (lane < 56) {
        *reinterpret_cast<u32x4*>(ldsu + L_XN + w * 256 + lane * 4) = xn;
        if (w == 1) *reinterpret_cast<u32x4*>(ldsu + L_X + lane * 4) = xv;
      }
    }
    STAMP(1);
    unsigned xp[4];
#pragma unroll
    for (int i = 0; i < 3; ++i) xp[i] = ldsu[L_XN + w * 256 + qrh * 112 + j0 + 32 * i];
    xp[3] = ldsu[L_XN + w * 256 + qrh * 112 + j3];
    xp[3] = (j0 < 16) ? xp[3] : 0u;
    asm volatile("s_waitcnt vmcnt(0)" : "+v"(Fq[0]), "+v"(Fq[1]), "+v"(Fq[2]), "+v"(Fq[3]), "+v"(Fq[4]), "+v"(Fq[5]), "+v"(Fq[6]), "+v"(Fq[7]));
    {
      float fa[8];
#pragma unroll
      for (int k = 0; k < 8; ++k) fa[k] = 0.f;
#pragma unroll
      for (int i = 0; i < 4; ++i)
#pragma unroll
        for (int k = 0; k < 4; ++k) {
          fa[2 * k] = dot2(perm_lo(Fq[2 * i][k], Fq[2 * i + 1][k]), xp[i], fa[2 * k]);
          fa[2 * k + 1] = dot2(perm_hi(Fq[2 * i][k], Fq[2 * i + 1][k]), xp[i], fa[2 * k + 1]);
        }
      float f4[4], f2[2];
#pragma unroll
      for (int k = 0; k < 4; ++k) f4[k] = sw32(fa[k], fa[k + 4]);
#pragma unroll
      for (int k = 0; k < 2; ++k) f2[k] = sw16(f4[k], f4[k + 2]);
      const float f1 = trs<XOR8, 8>(f2[0], f2[1], lane);
      lds[L_QP + (w - 1) * 64 + (lane & 7) * 8 + (lane >> 5) * 4 + ((lane >> 4) & 1) * 2 + ((lane >> 3) & 1)] = f1;
    }
    asm volatile("s_waitcnt lgkmcnt(0)" ::: "memory");
    if (lane == 0) __hip_atomic_fetch_add(ldsu + L_CNT, 1u, __ATOMIC_RELAXED, __HIP_MEMORY_SCOPE_WORKGROUP);
    STAMP(2);
    STAMP(3);
  }
  if (w != 0) issue_range<0>(R, cx, std::make_integer_sequence<int, KA>{});
  asm volatile("" ::: "memory");
  if (w == 0) bar();
  issue_range<KA>(R, cx, std::make_integer_sequence<int, WK - KA>{});
  STAMP(15);
  if (w != 0) bar();
  waitv<WK - KA>(ep);
  PIN1(cpf);
  const unsigned long long e64 = (((unsigned long long)ep[1] << 32) | ep[0]) + 1ull;
  const unsigned tlo = __builtin_amdgcn_readfirstlane((unsigned)e64);
  const unsigned thi_full = __builtin_amdgcn_readfirstlane((unsigned)(e64 >> 32));
  const unsigned thi = thi_full & 0xffu;
  const unsigned g0tag = tlo << 24, g1tag = (tlo >> 8) | (thi << 24);
  const u32x4 q4 = *reinterpret_cast<const u32x4*>(ldsu + L_Q + l32 * 4);
  STAMP(4);

  KState ks;
  u32x4 Fg[4], Fd[2];
  {
    const u32x4 rg = mkdesc(wg, D_ * F_ * 2), ru = mkdesc(wu, D_ * F_ * 2), rd = mkdesc(wd + (size_t)(slice * 16 + 2 * w) * D_, 2 * 896);
    const int fvo = (t >> 1) * (2 * F_ * 2) + slice * 32 + (t & 1) * 16;
    asm volatile("s_nop 4");
    ld_w<0>(Fg[0], fvo, rg); ld_w<F_ * 2>(Fg[1], fvo, rg);
    ld_w<0>(Fg[2], fvo, ru); ld_w<F_ * 2>(Fg[3], fvo, ru);
    ld_w<0>(Fd[0], cx.wvo, rd); ld_w<896>(Fd[1], cx.wvo, rd);
  }
  constexpr int C1 = FFNC;
  static_assert(idx_of_step(C1 - 1) >= WK && (C1 % BARN) == 0 && C1 + BARN <= 64, "ffn plan");
  kphase<0>(R, cx, q4, ks, lane, std::make_integer_sequence<int, C1>{});
  {
    asm volatile("" : "+v"(Fg[0]), "+v"(Fg[1]), "+v"(Fg[2]), "+v"(Fg[3]), "+v"(Fd[0]), "+v"(Fd[1]));
    const int jj = t >> 1;
    const unsigned xa = ldsu[L_XN + 256 + (jj < 224 ? jj : 0)];
    float fa[16];
#pragma unroll
    for (int k = 0; k < 4; ++k) {
      fa[2 * k] = dot2(perm_lo(Fg[0][k], Fg[1][k]), xa, 0.f);
      fa[2 * k + 1] = dot2(perm_hi(Fg[0][k], Fg[1][k]), xa, 0.f);
      fa[8 + 2 * k] = dot2(perm_lo(Fg[2][k], Fg[3][k]), xa, 0.f);
      fa[8 + 2 * k + 1] = dot2(perm_hi(Fg[2][k], Fg[3][k]), xa, 0.f);
    }
    float f8[8], f4[4];
    f32x2 f2;
#pragma unroll
    for (int k = 0; k < 8; ++k) f8[k] = sw32(fa[k], fa[k + 8]);
#pragma unroll
    for (int k = 0; k < 4; ++k) f4[k] = sw16(f8[k], f8[k + 4]);
#pragma unroll
    for (int k = 0; k < 2; ++k) { float v = trs<XOR8, 8>(f4[k], f4[k + 2], lane);
      v += dpp<XOR2>(v); v += dpp<XOR1>(dpp<XOR7>(v)) ; f2[k] = v; }
    if ((lane & 6) == 0)
      *reinterpret_cast<f32x2*>(lds + L_FRED + w * 64 + (lane >> 5) * 16 + (lane & 1) * 8 + ((lane >> 4) & 1) * 4 + ((lane >> 3) & 1) * 2) = f2;
    asm volatile("s_waitcnt lgkmcnt(0)" ::: "memory");
  }
  kphase<C1>(R, cx, q4, ks, lane, std::make_integer_sequence<int, BARN>{});
  {
    asm volatile("" ::: "memory");
    f32x2 gg = {0.f, 0.f}, uu = {0.f, 0.f};
#pragma unroll
    for (int ww = 0; ww < 8; ++ww) {
      gg += *reinterpret_cast<const f32x2*>(lds + L_FRED + ww * 64 + 2 * w);
      uu += *reinterpret_cast<const f32x2*>(lds + L_FRED + ww * 64 + 16 + 2 * w);
      if ((ww & 3) == 3) { PIN1(gg); PIN1(uu); }
    }
    float hm[2];
#pragma unroll
    for (int k = 0; k < 2; ++k) {
      const float gv = rbf(gg[k]), uv = rbf(uu[k]);
      const float sg = rbf(gv / (1.f + __expf(-gv)));
      hm[k] = sg * uv;
    }
    const unsigned hp = pk_bf16(hm[0], hm[1]);
    f32x4 z0, z1;
#pragma unroll
    for (int k = 0; k < 2; ++k) {
      z0[2 * k] = dot2(perm_lo(Fd[0][k], Fd[1][k]), hp, 0.f);
      z0[2 * k + 1] = dot2(perm_hi(Fd[0][k], Fd[1][k]), hp, 0.f);
      z1[2 * k] = dot2(perm_lo(Fd[0][k + 2], Fd[1][k + 2]), hp, 0.f);
      z1[2 * k + 1] = dot2(perm_hi(Fd[0][k + 2], Fd[1][k + 2]), hp, 0.f);
    }
    if (lane < 56) {
      *reinterpret_cast<f32x4*>(lds + L_YRED + w * 448 + lane * 8) = z0;
      *reinterpret_cast<f32x4*>(lds + L_YRED + w * 448 + lane * 8 + 4) = z1;
    }
    asm volatile("s_waitcnt lgkmcnt(0)" ::: "memory");
  }
  kphase<C1 + BARN>(R, cx, q4, ks, lane, std::make_integer_sequence<int, 64 - C1 - BARN>{});
  STAMP(5);
  const float mw = wave_max(fmaxf(ks.sc[0], ks.sc[1]));
  {
    const float p0 = __expf(ks.sc[0] - mw), p1 = __expf(ks.sc[1] - mw);
    const float lw = wave_sum(p0 + p1);
    const int li = ((lane >> 4) & 1) | ((lane >> 2) & 2) | (lane & 4) | ((lane << 2) & 8) | ((lane << 4) & 16);
    lds[L_P + w * 128 + half * 64 + li] = p0;
    lds[L_P + w * 128 + half * 64 + 32 + li] = p1;
    if (lane == 0) {
      u32x4 gr = {__float_as_uint(mw), tlo, __float_as_uint(lw), tlo};
      __builtin_amdgcn_raw_buffer_store_b128(gr, rs, WS_ML + ((b * H_ + h) * 64 + s * 8 + w) * 16, 0, 16);
    }
  }

  VState vs;
#pragma unroll
  for (int k = 0; k < 8; ++k) vs.acc[k] = 0.f;
  const int ml_off = WS_ML + ((b * H_ + h) * 64 + lane) * 16;
  const int z_off = WS_Z + ((b * 56 + ci) * 128) * 64 + t * 16;
  vphase<0>(R, cx, vs, lds + L_P + w * 128 + half * 64, rws, ml_off, z_off, std::make_integer_sequence<int, BARN>{});
  asm volatile("" ::: "memory");
  if (t < 224) {
    f32x2 zz = {0.f, 0.f};
#pragma unroll
    for (int ww = 0; ww < 8; ++ww) zz += *reinterpret_cast<const f32x2*>(lds + L_YRED + ww * 448 + 2 * t);
    const unsigned d0 = (__float_as_uint(zz[0]) + 0x80u) >> 8, d1 = (__float_as_uint(zz[1]) + 0x80u) >> 8;
    u32x4 gr = {d0 | g0tag, g1tag, d1 | g0tag, g1tag};
    __builtin_amdgcn_raw_buffer_store_b128(gr, rs, WS_Z + ((b * 56 + (t >> 2)) * 128 + zrow) * 64 + (t & 3) * 16, 0, 16);
  }
  vphase<BARN>(R, cx, vs, lds + L_P + w * 128 + half * 64, rws, ml_off, z_off, std::make_integer_sequence<int, 32 - BARN>{});
  STAMP(12);
  vphase<32>(R, cx, vs, lds + L_P + w * 128 + half * 64, rws, ml_off, z_off, std::make_integer_sequence<int, 16>{});
  STAMP(13);
  vphase<48>(R, cx, vs, lds + L_P + w * 128 + half * 64, rws, ml_off, z_off, std::make_integer_sequence<int, FSTEP - 48>{});
  static_assert(idx_of_kv(64 + FSTEP - 1) > WK + 64 + GATHER_STEP, "gather landed");
  float fscale;
  bool mlok;
  {
    PIN1(vs.mlg);
    mlok = __all(vs.mlg[1] == tlo && vs.mlg[3] == tlo);
    const float mi = __uint_as_float(vs.mlg[0]), li = __uint_as_float(vs.mlg[2]);
    const float M = wave_max(mi);
    const float L = wave_sum(li * __expf(mi - M));
    fscale = __expf(mw - M) / L;
    PIN1(fscale);
  }
  vphase<FSTEP>(R, cx, vs, lds + L_P + w * 128 + half * 64, rws, ml_off, z_off, std::make_integer_sequence<int, 64 - FSTEP>{});
  asm volatile("s_waitcnt vmcnt(0)" : "+v"(vs.zg));
  STAMP(6);
  if (!mlok) {
    unsigned spins = 0;
    u32x4 mg;
    do {
      ld_sc1(mg, ml_off, rws);
      waitv<0>(mg);
    } while (!__all(mg[1] == tlo && mg[3] == tlo) && ++spins < (1u << 18));
    const float mi = __uint_as_float(mg[0]), li = __uint_as_float(mg[2]);
    const float M = wave_max(mi);
    const float L = wave_sum(li * __expf(mi - M));
    fscale = __expf(mw - M) / L;
  }
  {
    f32x4 oh;
#pragma unroll
    for (int k = 0; k < 4; ++k) oh[k] = sw32(vs.acc[k], vs.acc[k + 4]) * fscale;
    *reinterpret_cast<f32x4*>(lds + L_ORED + w * 256 + l32 * 8 + half * 4) = oh;
  }
  STAMP(7);
  bar();
  STAMP(8);
  {
    float ov = 0.f;
#pragma unroll
    for (int ww = 0; ww < 8; ++ww) ov += lds[L_ORED + ww * 256 + w * 32 + l32];
    const float on = dpp<XOR1>(ov);
    const unsigned opk = pk_bf16(ov, on);
    float ya[8];
#pragma unroll
    for (int k = 0; k < 8; ++k) ya[k] = 0.f;
    ophase(R, opk, ya, std::make_integer_sequence<int, 16>{});
    if (lane < 56) {
      *reinterpret_cast<f32x4*>(lds + L_YRED + w * 448 + lane * 8) = f32x4{ya[0], ya[1], ya[2], ya[3]};
      *reinterpret_cast<f32x4*>(lds + L_YRED + w * 448 + lane * 8 + 4) = f32x4{ya[4], ya[5], ya[6], ya[7]};
    }
  }
  bar();
  if (t < 224) {
    f32x2 yy = {0.f, 0.f};
#pragma unroll
    for (int ww = 0; ww < 8; ++ww) yy += *reinterpret_cast<const f32x2*>(lds + L_YRED + ww * 448 + 2 * t);
    const unsigned d0 = (__float_as_uint(yy[0]) + 0x80u) >> 8, d1 = (__float_as_uint(yy[1]) + 0x80u) >> 8;
    u32x4 gr = {d0 | g0tag, g1tag, d1 | g0tag, g1tag};
    __builtin_amdgcn_raw_buffer_store_b128(gr, rs, WS_Y + ((b * 56 + (t >> 2)) * 128 + pidx) * 64 + (t & 3) * 16, 0, 16);
  }
  STAMP(9);
  if (!is_cons) return;

  {
    const int y_off = WS_Y + ((b * 56 + ci) * 128) * 64 + t * 16;
    u32x4 yg;
    ld_sc1(yg, y_off, rws);
    const bool zlive = t < 448;
    u32x4 zg = vs.zg;
    unsigned spins = 0;
    while (!__all(!zlive || ((zg[0] & 0xff000000u) == g0tag && zg[1] == g1tag && (zg[2] & 0xff000000u) == g0tag && zg[3] == g1tag)) && ++spins < (1u << 18)) {
      __builtin_amdgcn_s_sleep(1);
      ld_sc1(zg, z_off, rws);
      waitv<0>(zg);
    }
    float z0 = zlive ? __uint_as_float(zg[0] << 8) : 0.f, z1 = zlive ? __uint_as_float(zg[2] << 8) : 0.f;
    z0 += dpp<ROR4>(z0); z0 += dpp<XOR8>(z0); z0 = x16_sum(z0); z0 = x32_sum(z0);
    z1 += dpp<ROR4>(z1); z1 += dpp<XOR8>(z1); z1 = x16_sum(z1); z1 = x32_sum(z1);
    PIN1(z0); PIN1(z1);
    waitv<0>(yg);
    spins = 0;
    for (;;) {
      const bool ok = (yg[0] & 0xff000000u) == g0tag && yg[1] == g1tag && (yg[2] & 0xff000000u) == g0tag && yg[3] == g1tag;
      if (__all(ok) || ++spins >= (1u << 18)) break;
      __builtin_amdgcn_s_sleep(YSLEEP);
      if (!ok) ld_sc1_re(yg, y_off, rws);
    }
    float y0 = __uint_as_float(yg[0] << 8), y1 = __uint_as_float(yg[2] << 8);
    y0 += dpp<ROR4>(y0); y0 += dpp<XOR8>(y0); y0 = x16_sum(y0); y0 = x32_sum(y0);
    y1 += dpp<ROR4>(y1); y1 += dpp<XOR8>(y1); y1 = x16_sum(y1); y1 = x32_sum(y1);
    if (lane < 4) {
      *reinterpret_cast<f32x2*>(lds + L_C + w * 16 + lane * 2) = f32x2{y0, y1};
      *reinterpret_cast<f32x2*>(lds + L_C + w * 16 + 8 + lane * 2) = f32x2{z0, z1};
    }
    STAMP(10);
    bar();
    if (t < 8) {
      float a = 0.f, f = 0.f;
#pragma unroll
      for (int ww = 0; ww < 8; ++ww) { a += lds[L_C + ww * 16 + t]; f += lds[L_C + ww * 16 + 8 + t]; }
      const unsigned xo = reinterpret_cast<const u16*>(ldsu + L_X)[ci * 8 + t];
      const float xr = __uint_as_float(xo << 16);
      const float res = rbf(xr + rbf(a)) + rbf(f);
      out[b * D_ + ci * 8 + t] = __builtin_bit_cast(u16, (__bf16)res);
    }
    STAMP(11);
    if (ci == 0 && t == 0) {
      u32x2 ne = {tlo, thi_full};
      __builtin_amdgcn_raw_buffer_store_b64(ne, rs, WS_EPOCH + b * 256, 0, 16);
    }
  }
}

extern "C" void kernel_launch(void* const* d_in, const int* in_sizes, int n_in,
                              void* d_out, int out_size, void* d_ws, size_t ws_size,
                              hipStream_t stream) {
  const u16* x = (const u16*)d_in[0];
  const u16* k = (const u16*)d_in[1];
  const u16* v = (const u16*)d_in[2];
  const u16* wq = (const u16*)d_in[3];
  const u16* wo = (const u16*)d_in[4];
  const u16* wg = (const u16*)d_in[6];
  const u16* wu = (const u16*)d_in[7];
  const u16* wd = (const u16*)d_in[8];
  fused_kernel<<<NWG, NTHR, 0, stream>>>(x, k, v, wq, wo, wg, wu, wd, (u16*)d_out, (unsigned char*)d_ws);
}
```

```cpp
#include <hip/hip_runtime.h>
#include <hip/hip_bf16.h>
#include <stdint.h>
#include <utility>

typedef unsigned short u16;
typedef unsigned u32x4 __attribute__((ext_vector_type(4)));
typedef unsigned u32x2 __attribute__((ext_vector_type(2)));
typedef float f32x4 __attribute__((ext_vector_type(4)));
typedef float f32x2 __attribute__((ext_vector_type(2)));
typedef __bf16 bf16x2 __attribute__((ext_vector_type(2)));

constexpr int D_ = 448, H_ = 16, SK = 8192, DH = 256, F_ = 1792;
constexpr int NWG = 256, NTHR = 512;
constexpr int KEYS_WG = 1024;

constexpr int WK = 44;
constexpr int KA = 16;
constexpr int WOP = 3;
constexpr int NISS = 160;
#define GUSTEP 44
#define MLSTEP 40
constexpr int FSTEP = 60;
constexpr int nwo_before(int c) { return c / WOP < 32 ? c / WOP : 32; }
constexpr bool is_wo_step(int c) { return (c % WOP) == WOP - 1 && c / WOP < 32; }
constexpr int kv_of_step(int c) { return WK + c - nwo_before(c); }
constexpr int idx_of_kv(int m) {
  if (m < WK) return m;
  for (int c = 0; c < NISS; ++c) if (!is_wo_step(c) && kv_of_step(c) == m) return WK + c;
  return -1;
}
constexpr int idx_of_wo(int k) { return WK + k * WOP + WOP - 1; }
constexpr int slot_of(int n) { return n < WK ? n : slot_of(idx_of_kv(n - WK)); }
constexpr int wait_of_step(int c) { return ((WK + c < NISS) ? WK + c : NISS) - idx_of_kv(c) - 1; }
static_assert(idx_of_kv(127) < NISS && idx_of_kv(127) > 0 && idx_of_wo(31) < NISS && WK > 32 && KA <= 16, "plan");

constexpr int WS_EPOCH = 0;
constexpr int WS_ML = 4096;
constexpr int WS_Q = WS_ML + 2 * 16 * 64 * 16;
constexpr int WS_Y = WS_Q + 2 * 16 * 4096;
constexpr int WS_Z = WS_Y + 2 * 56 * 128 * 64;
constexpr int WS_GU = WS_Z + 2 * 56 * 128 * 64;
constexpr int WS_END = WS_GU + 2 * 128 * 1024;

constexpr int L_XN = 0;
constexpr int L_ORED = L_XN + 5 * 256;
constexpr int L_FRED = L_ORED + 8 * 256;
constexpr int L_YRED = L_FRED + 8 * 128;
constexpr int L_P = L_YRED + 8 * 448;
constexpr int L_C = L_P + 8 * 128;
constexpr int L_X = L_C + 8 * 16;
constexpr int L_Q = L_X + 256;
constexpr int L_CNT = L_Q + 128;
constexpr int L_QP = L_CNT + 4;
constexpr int L_END = L_QP + 256;

constexpr unsigned SPIN_MAX = 1u << 18;

__device__ __forceinline__ float dot2(unsigned a, unsigned b, float c) {
  return __builtin_amdgcn_fdot2_f32_bf16(__builtin_bit_cast(bf16x2, a), __builtin_bit_cast(bf16x2, b), c, false);
}
__device__ __forceinline__ float bflo(unsigned v) { return __uint_as_float(v << 16); }
__device__ __forceinline__ float bfhi(unsigned v) { return __uint_as_float(v & 0xffff0000u); }
__device__ __forceinline__ unsigned pk_bf16(float lo, float hi) {
  bf16x2 r; r[0] = (__bf16)lo; r[1] = (__bf16)hi; return __builtin_bit_cast(unsigned, r);
}
__device__ __forceinline__ float rbf(float x) { return (float)(__bf16)x; }
__device__ __forceinline__ unsigned perm_lo(unsigned a, unsigned b) { return __builtin_amdgcn_perm(b, a, 0x05040100u); }
__device__ __forceinline__ unsigned perm_hi(unsigned a, unsigned b) { return __builtin_amdgcn_perm(b, a, 0x07060302u); }
template <int CTRL> __device__ __forceinline__ float dpp(float x) {
  return __builtin_bit_cast(float, __builtin_amdgcn_mov_dpp(__builtin_bit_cast(int, x), CTRL, 0xf, 0xf, true));
}
constexpr int XOR1 = 0xB1, XOR2 = 0x4E, XOR7 = 0x141, XOR8 = 0x128, ROR4 = 0x124;
__device__ __forceinline__ float sw16(float a, float b) {
  auto s = __builtin_amdgcn_permlane16_swap(__float_as_uint(a), __float_as_uint(b), false, false);
  return __uint_as_float(s[0]) + __uint_as_float(s[1]);
}
__device__ __forceinline__ float sw32(float a, float b) {
  auto s = __builtin_amdgcn_permlane32_swap(__float_as_uint(a), __float_as_uint(b), false, false);
  return __uint_as_float(s[0]) + __uint_as_float(s[1]);
}
__device__ __forceinline__ float x16_sum(float x) { return sw16(x, x); }
__device__ __forceinline__ float x32_sum(float x) { return sw32(x, x); }
__device__ __forceinline__ float x16_max(float x) {
  auto s = __builtin_amdgcn_permlane16_swap(__float_as_uint(x), __float_as_uint(x), false, false);
  return fmaxf(__uint_as_float(s[0]), __uint_as_float(s[1]));
}
__device__ __forceinline__ float x32_max(float x) {
  auto s = __builtin_amdgcn_permlane32_swap(__float_as_uint(x), __float_as_uint(x), false, false);
  return fmaxf(__uint_as_float(s[0]), __uint_as_float(s[1]));
}
__device__ __forceinline__ float wave_sum(float x) {
  x += dpp<XOR1>(x); x += dpp<XOR2>(x); x += dpp<XOR7>(x); x += dpp<XOR8>(x);
  x = x16_sum(x); x = x32_sum(x); return x;
}
__device__ __forceinline__ float wave_max(float x) {
  x = fmaxf(x, dpp<XOR1>(x)); x = fmaxf(x, dpp<XOR2>(x)); x = fmaxf(x, dpp<XOR7>(x)); x = fmaxf(x, dpp<XOR8>(x));
  x = x16_max(x); x = x32_max(x); return x;
}
template <int CTRL, int BIT> __device__ __forceinline__ float trs(float lo, float hi, int lane) {
  const float t1 = lo + dpp<CTRL>(lo), t2 = hi + dpp<CTRL>(hi);
  return (lane & BIT) ? t2 : t1;
}
__device__ __forceinline__ void bar() { asm volatile("s_waitcnt lgkmcnt(0)\n\ts_barrier" ::: "memory"); }
#define PIN8(a_) asm volatile("" : "+v"(a_[0]), "+v"(a_[1]), "+v"(a_[2]), "+v"(a_[3]), "+v"(a_[4]), "+v"(a_[5]), "+v"(a_[6]), "+v"(a_[7]))
#define PIN1(a_) asm volatile("" : "+v"(a_))
#define CFENCE() asm volatile("" ::: "memory")
#define LDS_DRAIN() asm volatile("s_waitcnt lgkmcnt(0)" ::: "memory")
#define STAMP(i_)

__device__ __forceinline__ u32x4 mkdesc(const void* p, unsigned bytes) {
  const unsigned long long a = (unsigned long long)p;
  u32x4 d;
  d[0] = __builtin_amdgcn_readfirstlane((unsigned)a);
  d[1] = __builtin_amdgcn_readfirstlane((unsigned)(a >> 32) & 0xffffu);
  d[2] = __builtin_amdgcn_readfirstlane(bytes);
  d[3] = 0x00020000u;
  return d;
}
template <int OFF> __device__ __forceinline__ void ld_nt_new(u32x4& d, int voff, const u32x4& rsrc, int soff) {
  asm volatile("buffer_load_dwordx4 %0, %1, %2, %3 offen offset:%4 nt" : "=v"(d) : "v"(voff), "s"(rsrc), "s"(soff), "i"(OFF));
}
template <int OFF> __device__ __forceinline__ void ld_nt_re(u32x4& d, int voff, const u32x4& rsrc, int soff) {
  asm volatile("buffer_load_dwordx4 %0, %1, %2, %3 offen offset:%4 nt" : "+v"(d) : "v"(voff), "s"(rsrc), "s"(soff), "i"(OFF));
}
template <int OFF> __device__ __forceinline__ void ld_pl_re(u32x4& d, int voff, const u32x4& rsrc, int soff) {
  asm volatile("buffer_load_dwordx4 %0, %1, %2, %3 offen offset:%4" : "+v"(d) : "v"(voff), "s"(rsrc), "s"(soff), "i"(OFF));
}
template <int OFF> __device__ __forceinline__ void ld_w(u32x4& d, int voff, const u32x4& rsrc) {
  asm volatile("buffer_load_dwordx4 %0, %1, %2, 0 offen offset:%3" : "=v"(d) : "v"(voff), "s"(rsrc), "i"(OFF));
}
__device__ __forceinline__ void ld_ws(u32x4& d, int voff, const u32x4& rsrc, int soff) {
  asm volatile("buffer_load_dwordx4 %0, %1, %2, %3 offen" : "=v"(d) : "v"(voff), "s"(rsrc), "s"(soff));
}
__device__ __forceinline__ void ld_sc1(u32x4& d, int voff, const u32x4& rsrc) {
  asm volatile("buffer_load_dwordx4 %0, %1, %2, 0 offen sc1" : "=v"(d) : "v"(voff), "s"(rsrc) : "memory");
}
__device__ __forceinline__ void ld_sc1_re(u32x4& d, int voff, const u32x4& rsrc) {
  asm volatile("buffer_load_dwordx4 %0, %1, %2, 0 offen sc1\n\ts_waitcnt vmcnt(0)" : "+v"(d) : "v"(voff), "s"(rsrc) : "memory");
}
template <int N> __device__ __forceinline__ void waitv(u32x4& d) { asm volatile("s_waitcnt vmcnt(%1)" : "+v"(d) : "i"(N)); }

struct Ctx {
  u32x4 rk, rv, ro;
  int lo16, wvo;
};
template <int n> __device__ __forceinline__ void issue(u32x4 (&R)[WK], const Ctx& c) {
  if constexpr (n < NISS) {
    constexpr int sl = slot_of(n);
    if constexpr (n < WK) {
      if constexpr (n >= KA) ld_nt_new<(n & 3) * 1024>(R[sl], c.lo16, c.rk, (n >> 2) * 32768);
      else ld_nt_re<(n & 3) * 1024>(R[sl], c.lo16, c.rk, (n >> 2) * 32768);
    } else if constexpr (is_wo_step(n - WK)) {
      constexpr int k = nwo_before(n - WK);
      ld_pl_re<(k & 3) * 896>(R[sl], c.wvo, c.ro, (k >> 2) * 3584);
    } else {
      constexpr int m = kv_of_step(n - WK);
      if constexpr (m < 64) ld_nt_re<(m & 3) * 1024>(R[sl], c.lo16, c.rk, (m >> 2) * 32768);
      else ld_nt_re<((m - 64) & 3) * 1024>(R[sl], c.lo16, c.rv, ((m - 64) >> 2) * 32768);
    }
  }
}
template <int A, int... I> __device__ __forceinline__ void issue_range(u32x4 (&R)[WK], const Ctx& c, std::integer_sequence<int, I...>) {
  (issue<A + I>(R, c), ...);
}

struct KState { float t0, t1, t2, t3, t4; float sc[2]; };
template <int c> __device__ __forceinline__ void kstep(u32x4 (&R)[WK], const Ctx& cx, const u32x4& q4, KState& st, int lane) {
  constexpr int sl = slot_of(idx_of_kv(c));
  waitv<wait_of_step(c)>(R[sl]);
  float d = dot2(R[sl][0], q4[0], 0.f);
  d = dot2(R[sl][1], q4[1], d);
  d = dot2(R[sl][2], q4[2], d);
  d = dot2(R[sl][3], q4[3], d);
  PIN1(d);
  issue<WK + c>(R, cx);
  if constexpr ((c & 7) == 7) __builtin_amdgcn_s_barrier();
  constexpr int i = c & 31;
  if constexpr ((i & 1) == 0) { st.t0 = d; return; }
  else {
    d = sw16(st.t0, d);
    if constexpr ((i & 2) == 0) { st.t1 = d; return; }
    else {
      d = trs<XOR8, 8>(st.t1, d, lane);
      if constexpr ((i & 4) == 0) { st.t2 = d; return; }
      else {
        d = trs<XOR7, 4>(st.t2, d, lane);
        if constexpr ((i & 8) == 0) { st.t3 = d; return; }
        else {
          d = trs<XOR2, 2>(st.t3, d, lane);
          if constexpr ((i & 16) == 0) { st.t4 = d; return; }
          else {
            d = trs<XOR1, 1>(st.t4, d, lane);
            st.sc[c >> 5] = rbf(d) * 0.0625f;
          }
        }
      }
    }
  }
}
template <int A, int... I> __device__ __forceinline__ void kphase(u32x4 (&R)[WK], const Ctx& cx, const u32x4& q4, KState& st, int lane, std::integer_sequence<int, I...>) {
  (kstep<A + I>(R, cx, q4, st, lane), ...);
}

struct VState { float acc[8]; f32x4 p4; u32x4 mlg, zg; };
template <int j> __device__ __forceinline__ void vstep(u32x4 (&R)[WK], const Ctx& cx, VState& st, const float* pl, const u32x4& rws, const u32x4& rwz, int ml_off, int z_off) {
  constexpr int c = 64 + j;
  constexpr int sl = slot_of(idx_of_kv(c));
  if constexpr ((j & 3) == 0) st.p4 = *reinterpret_cast<const f32x4*>(pl + j);
  waitv<wait_of_step(c)>(R[sl]);
  const float p = st.p4[j & 3];
#pragma unroll
  for (int k = 0; k < 4; ++k) {
    st.acc[2 * k] = fmaf(p, bflo(R[sl][k]), st.acc[2 * k]);
    st.acc[2 * k + 1] = fmaf(p, bfhi(R[sl][k]), st.acc[2 * k + 1]);
  }
  PIN8(st.acc);
  issue<WK + c>(R, cx);
  if constexpr (j == MLSTEP) { ld_sc1(st.mlg, ml_off, rws); ld_sc1(st.zg, z_off, rwz); }
  if constexpr (j >= 48) {
    constexpr int i = j - 48;
    static_assert(idx_of_kv(64 + j) > idx_of_wo(2 * i + 1), "wo pair landed");
    constexpr int sa = slot_of(idx_of_wo(2 * i)), sb = slot_of(idx_of_wo(2 * i + 1));
    PIN1(R[sa]); PIN1(R[sb]);
#pragma unroll
    for (int k = 0; k < 4; ++k) {
      const unsigned a = R[sa][k], e = R[sb][k];
      R[sa][k] = perm_lo(a, e); R[sb][k] = perm_hi(a, e);
    }
    PIN1(R[sa]); PIN1(R[sb]);
  }
  if constexpr ((j & 7) == 7 && j < 48) __builtin_amdgcn_s_barrier();
}
template <int A, int... I> __device__ __forceinline__ void vphase(u32x4 (&R)[WK], const Ctx& cx, VState& st, const float* pl, const u32x4& rws, const u32x4& rwz, int ml_off, int z_off, std::integer_sequence<int, I...>) {
  (vstep<A + I>(R, cx, st, pl, rws, rwz, ml_off, z_off), ...);
}
template <int i> __device__ __forceinline__ void ostep(u32x4 (&R)[WK], unsigned opk, float (&ya)[8]) {
  constexpr int sa = slot_of(idx_of_wo(2 * i)), sb = slot_of(idx_of_wo(2 * i + 1));
  const unsigned so = __builtin_amdgcn_readlane(opk, 2 * i);
#pragma unroll
  for (int k = 0; k < 4; ++k) {
    ya[2 * k] = dot2(R[sa][k], so, ya[2 * k]);
    ya[2 * k + 1] = dot2(R[sb][k], so, ya[2 * k + 1]);
  }
}
template <int... I> __device__ __forceinline__ void ophase(u32x4 (&R)[WK], unsigned opk, float (&ya)[8], std::integer_sequence<int, I...>) {
  (ostep<I>(R, opk, ya), ...);
}
#define SEQ(n_) std::make_integer_sequence<int, (n_)>{}

__global__ void __launch_bounds__(NTHR, 2)
fused_kernel(const u16* __restrict__ x, const u16* __restrict__ kc, const u16* __restrict__ vc,
             const u16* __restrict__ wq, const u16* __restrict__ wo, const u16* __restrict__ wg,
             const u16* __restrict__ wu, const u16* __restrict__ wd, u16* __restrict__ out,
             unsigned char* ws) {
  __shared__ __attribute__((aligned(16))) float lds[L_END];
  unsigned* ldsu = reinterpret_cast<unsigned*>(lds);
  const int t = threadIdx.x, lane = t & 63;
  const int w = __builtin_amdgcn_readfirstlane(t >> 6);
  const int g = blockIdx.x;
  const int xcd = g & 7, r = g >> 3, hsel = (r >> 3) & 1, b = r >> 4, s = r & 7;
  const int h = xcd + 8 * hsel;
  const int i16 = hsel * 8 + s;
  const int pidx = h * 8 + s;
  const bool has_ffn = i16 < 14;
  const int slice = xcd * 14 + (has_ffn ? i16 : 13);
  const int zrow = has_ffn ? slice : (112 + xcd * 2 + (i16 - 14));
  const bool is_cons = i16 >= 9;
  const int ci = xcd * 7 + (is_cons ? (i16 - 9) : 0);
  const int half = lane >> 5, l32 = lane & 31;
  const int l56 = lane < 56 ? lane : lane - 8;

  const __amdgpu_buffer_rsrc_t rs = __builtin_amdgcn_make_buffer_rsrc((void*)ws, 0, WS_END, 0x00020000);
  const u32x4 rws = mkdesc(ws, WS_END);
  const size_t kvoff = ((size_t)(b * H_ + h) * SK + s * KEYS_WG + w * 8) * DH;
  Ctx cx;
  cx.rk = mkdesc(kc + kvoff, 524288 - w * 4096);
  cx.rv = mkdesc(vc + kvoff, 524288 - w * 4096);
  cx.ro = mkdesc(wo + (size_t)(h * 256 + w * 32) * D_, 32 * 896);
  cx.lo16 = lane * 16;
  cx.wvo = l56 * 16;

  STAMP(0);
  u32x4 R[WK];
#pragma unroll
  for (int i = 0; i < KA; ++i) asm volatile("" : "=v"(R[i]));
  if (t == 0) ldsu[L_CNT] = 0u;
  bar();
  u32x4 ep;
  asm volatile("s_nop 4");
  ld_sc1(ep, WS_EPOCH + b * 256, rws);
  u32x4 xv, Fq[8];
  const bool worker = (w >= 1 && w <= 4);
  const int qline = xcd * 8 + (i16 >> 1), qrh = i16 & 1;
  const int j0 = ((t - 64) >> 3) & 31;
  if (w == 0) {
    issue_range<0>(R, cx, SEQ(KA));
  } else if (worker) {
    const u32x4 rx = mkdesc(x + b * D_, D_ * 2);
    const u32x4 rq = mkdesc(wq + (size_t)(qrh * 224) * 4096 + qline * 64, 223 * 8192 + 128);
    const int qvo = j0 * 16384 + (lane & 7) * 16;
    asm volatile("s_nop 4");
    ld_ws(xv, cx.wvo, rx, 0);
#pragma unroll
    for (int i = 0; i < 4; ++i) {
      ld_ws(Fq[2 * i], qvo, rq, i * 524288);
      ld_ws(Fq[2 * i + 1], qvo, rq, i * 524288 + 8192);
    }
  }
  CFENCE();
  if (w == 0) {
    waitv<KA>(ep);
    const unsigned tl0 = __builtin_amdgcn_readfirstlane(ep[0]) + 1u;
    {
      unsigned spins = 0;
      while (__hip_atomic_load(ldsu + L_CNT, __ATOMIC_RELAXED, __HIP_MEMORY_SCOPE_WORKGROUP) != 4u && ++spins < (SPIN_MAX << 4)) __builtin_amdgcn_s_sleep(1);
      CFENCE();
    }
    __builtin_amdgcn_s_setprio(3);
    if (lane < 32) {
      f32x2 a = {0.f, 0.f};
#pragma unroll
      for (int ww = 0; ww < 4; ++ww) a += *reinterpret_cast<const f32x2*>(lds + L_QP + ww * 64 + lane * 2);
      u32x4 gr = {__float_as_uint(a[0]), tl0, __float_as_uint(a[1]), tl0};
      __builtin_amdgcn_raw_buffer_store_b128(gr, rs, WS_Q + ((b * H_ + (qline >> 2)) * 2 + qrh) * 2048 + ((qline & 3) * 64 + lane * 2) * 8, 0, 16);
    }
    const int q_off = WS_Q + (b * H_ + h) * 4096 + lane * 32;
    u32x4 qa0, qa1, qb0, qb1;
    unsigned spins = 0;
    bool okq;
    do {
      ld_sc1(qa0, q_off, rws); ld_sc1(qa1, q_off + 16, rws); ld_sc1(qb0, q_off + 2048, rws); ld_sc1(qb1, q_off + 2064, rws);
      asm volatile("s_waitcnt vmcnt(0)" : "+v"(qa0), "+v"(qa1), "+v"(qb0), "+v"(qb1));
      okq = qa0[1] == tl0 && qa0[3] == tl0 && qa1[1] == tl0 && qa1[3] == tl0 && qb0[1] == tl0 && qb0[3] == tl0 && qb1[1] == tl0 && qb1[3] == tl0;
    } while (!__all(okq) && ++spins < SPIN_MAX);
    *reinterpret_cast<u32x2*>(ldsu + L_Q + lane * 2) = u32x2{
        pk_bf16(__uint_as_float(qa0[0]) + __uint_as_float(qb0[0]), __uint_as_float(qa0[2]) + __uint_as_float(qb0[2])),
        pk_bf16(__uint_as_float(qa1[0]) + __uint_as_float(qb1[0]), __uint_as_float(qa1[2]) + __uint_as_float(qb1[2]))};
    __builtin_amdgcn_s_setprio(0);
  } else if (worker) {
    waitv<8>(xv);
    {
      float xf[8];
#pragma unroll
      for (int k = 0; k < 4; ++k) { xf[2 * k] = bflo(xv[k]); xf[2 * k + 1] = bfhi(xv[k]); }
      float sm = 0.f;
#pragma unroll
      for (int k = 0; k < 8; ++k) sm += xf[k];
      sm = lane < 56 ? sm : 0.f;
      sm = wave_sum(sm);
      const float mexact = sm / (float)D_;
      const float m = rbf(mexact);
      float vs = 0.f;
#pragma unroll
      for (int k = 0; k < 8; ++k) { float d = xf[k] - mexact; vs += d * d; }
      vs = lane < 56 ? vs : 0.f;
      vs = wave_sum(vs);
      const float var = rbf(vs / (float)D_);
      const float den = rbf(sqrtf(rbf(var + 1e-5f)));
      u32x4 xn;
#pragma unroll
      for (int k = 0; k < 4; ++k) xn[k] = pk_bf16(rbf(xf[2 * k] - m) / den, rbf(xf[2 * k + 1] - m) / den);
      if (lane < 56) {
        *reinterpret_cast<u32x4*>(ldsu + L_XN + w * 256 + lane * 4) = xn;
        if (w == 1) *reinterpret_cast<u32x4*>(ldsu + L_X + lane * 4) = xv;
      }
    }
    unsigned xp[4];
#pragma unroll
    for (int i = 0; i < 3; ++i) xp[i] = ldsu[L_XN + w * 256 + qrh * 112 + j0 + 32 * i];
    xp[3] = ldsu[L_XN + w * 256 + qrh * 112 + (j0 < 16 ? j0 + 96 : j0)];
    xp[3] = (j0 < 16) ? xp[3] : 0u;
    asm volatile("s_waitcnt vmcnt(0)" : "+v"(Fq[0]), "+v"(Fq[1]), "+v"(Fq[2]), "+v"(Fq[3]), "+v"(Fq[4]), "+v"(Fq[5]), "+v"(Fq[6]), "+v"(Fq[7]));
    {
      float fa[8];
#pragma unroll
      for (int k = 0; k < 8; ++k) fa[k] = 0.f;
#pragma unroll
      for (int i = 0; i < 4; ++i)
#pragma unroll
        for (int k = 0; k < 4; ++k) {
          fa[2 * k] = dot2(perm_lo(Fq[2 * i][k], Fq[2 * i + 1][k]), xp[i], fa[2 * k]);
          fa[2 * k + 1] = dot2(perm_hi(Fq[2 * i][k], Fq[2 * i + 1][k]), xp[i], fa[2 * k + 1]);
        }
      float f4[4], f2[2];
#pragma unroll
      for (int k = 0; k < 4; ++k) f4[k] = sw32(fa[k], fa[k + 4]);
#pragma unroll
      for (int k = 0; k < 2; ++k) f2[k] = sw16(f4[k], f4[k + 2]);
      const float f1 = trs<XOR8, 8>(f2[0], f2[1], lane);
      lds[L_QP + (w - 1) * 64 + (lane & 7) * 8 + (lane >> 5) * 4 + ((lane >> 4) & 1) * 2 + ((lane >> 3) & 1)] = f1;
    }
    LDS_DRAIN();
    if (lane == 0) __hip_atomic_fetch_add(ldsu + L_CNT, 1u, __ATOMIC_RELAXED, __HIP_MEMORY_SCOPE_WORKGROUP);
  }
  if (w != 0) issue_range<0>(R, cx, SEQ(KA));
  CFENCE();
  if (w == 0) bar();
  u32x4 Fg[4], Fd[2];
  {
    const u32x4 rg = mkdesc(wg + (size_t)((slice & 3) * 112) * F_ + (slice >> 2) * 64, 111 * F_ * 2 + 128);
    const u32x4 ru = mkdesc(wu + (size_t)((slice & 3) * 112) * F_ + (slice >> 2) * 64, 111 * F_ * 2 + 128);
    const u32x4 rd = mkdesc(wd + (size_t)(slice * 16 + 2 * w) * D_, 2 * 896);
    const int fvo = (t >> 3) * (2 * F_ * 2) + (t & 7) * 16;
    asm volatile("s_nop 4");
    ld_w<0>(Fg[0], fvo, rg); ld_w<F_ * 2>(Fg[1], fvo, rg);
    ld_w<0>(Fg[2], fvo, ru); ld_w<F_ * 2>(Fg[3], fvo, ru);
    ld_w<0>(Fd[0], cx.wvo, rd); ld_w<896>(Fd[1], cx.wvo, rd);
  }
  issue_range<KA>(R, cx, SEQ(WK - KA));
  if (w != 0) bar();
  waitv<WK - KA>(ep);
  const unsigned long long e64 = (((unsigned long long)ep[1] << 32) | ep[0]) + 1ull;
  const unsigned tlo = __builtin_amdgcn_readfirstlane((unsigned)e64);
  const unsigned thi_full = __builtin_amdgcn_readfirstlane((unsigned)(e64 >> 32));
  const unsigned g0tag = tlo << 24, g1tag = (tlo >> 8) | ((thi_full & 0xffu) << 24);
  const u32x4 q4 = *reinterpret_cast<const u32x4*>(ldsu + L_Q + l32 * 4);

  KState ks;
  kphase<0>(R, cx, q4, ks, lane, SEQ(24));
  {
    asm volatile("" : "+v"(Fg[0]), "+v"(Fg[1]), "+v"(Fg[2]), "+v"(Fg[3]), "+v"(Fd[0]), "+v"(Fd[1]));
    const int jj = t >> 3;
    const unsigned xa = ldsu[L_XN + 256 + (slice & 3) * 56 + (jj < 56 ? jj : 0)];
    float fa[16];
#pragma unroll
    for (int k = 0; k < 4; ++k) {
      fa[2 * k] = dot2(perm_lo(Fg[0][k], Fg[1][k]), xa, 0.f);
      fa[2 * k + 1] = dot2(perm_hi(Fg[0][k], Fg[1][k]), xa, 0.f);
      fa[8 + 2 * k] = dot2(perm_lo(Fg[2][k], Fg[3][k]), xa, 0.f);
      fa[8 + 2 * k + 1] = dot2(perm_hi(Fg[2][k], Fg[3][k]), xa, 0.f);
    }
    float f8[8], f4[4];
    f32x2 f2;
#pragma unroll
    for (int k = 0; k < 8; ++k) f8[k] = sw32(fa[k], fa[k + 8]);
#pragma unroll
    for (int k = 0; k < 4; ++k) f4[k] = sw16(f8[k], f8[k + 4]);
#pragma unroll
    for (int k = 0; k < 2; ++k) f2[k] = trs<XOR8, 8>(f4[k], f4[k + 2], lane);
    *reinterpret_cast<f32x2*>(lds + L_FRED + w * 128 + (lane >> 5) * 64 + (lane & 7) * 8 + ((lane >> 4) & 1) * 4 + ((lane >> 3) & 1) * 2) = f2;
    LDS_DRAIN();
  }
  kphase<24>(R, cx, q4, ks, lane, SEQ(8));
  CFENCE();
  if (w == 7) {
    f32x2 a = {0.f, 0.f};
#pragma unroll
    for (int ww = 0; ww < 7; ++ww) a += *reinterpret_cast<const f32x2*>(lds + L_FRED + ww * 128 + lane * 2);
    u32x4 gr = {__float_as_uint(a[0]), tlo, __float_as_uint(a[1]), tlo};
    __builtin_amdgcn_raw_buffer_store_b128(gr, rs, WS_GU + (b * 128 + zrow) * 1024 + lane * 16, 0, 16);
  }
  kphase<32>(R, cx, q4, ks, lane, SEQ(GUSTEP - 32));
  u32x4 gu;
  const int gu_off = WS_GU + (b * 128 + (slice & ~3) + (lane >> 4)) * 1024 + (((lane >> 3) & 1) * 64 + (slice & 3) * 16 + (lane & 7) * 2) * 8;
  ld_sc1(gu, gu_off, rws);
  kphase<GUSTEP>(R, cx, q4, ks, lane, SEQ(64 - GUSTEP));

  const float mw = wave_max(fmaxf(ks.sc[0], ks.sc[1]));
  {
    const float p0 = __expf(ks.sc[0] - mw), p1 = __expf(ks.sc[1] - mw);
    const float lw = wave_sum(p0 + p1);
    const int li = ((lane >> 4) & 1) | ((lane >> 2) & 2) | (lane & 4) | ((lane << 2) & 8) | ((lane << 4) & 16);
    lds[L_P + w * 128 + half * 64 + li] = p0;
    lds[L_P + w * 128 + half * 64 + 32 + li] = p1;
    if (lane == 0) {
      u32x4 gr = {__float_as_uint(mw), tlo, __float_as_uint(lw), tlo};
      __builtin_amdgcn_raw_buffer_store_b128(gr, rs, WS_ML + ((b * H_ + h) * 64 + s * 8 + w) * 16, 0, 16);
    }
  }

  VState vs;
#pragma unroll
  for (int k = 0; k < 8; ++k) vs.acc[k] = 0.f;
  const u32x4 rwz = mkdesc(ws, is_cons ? WS_END : 0);
  const int ml_off = WS_ML + ((b * H_ + h) * 64 + lane) * 16;
  const int z_off = WS_Z + ((b * 56 + ci) * 128) * 64 + t * 16;
  const float* pl = lds + L_P + w * 128 + half * 64;
  vphase<0>(R, cx, vs, pl, rws, rwz, ml_off, z_off, SEQ(16));
  static_assert(idx_of_kv(64 + 15) >= WK + GUSTEP, "gate/up gather landed");
  {
    PIN1(gu);
    if (!__all(gu[1] == tlo && gu[3] == tlo)) {
      unsigned spins = 0;
      do {
        ld_sc1(gu, gu_off, rws);
        waitv<0>(gu);
      } while (!__all(gu[1] == tlo && gu[3] == tlo) && ++spins < SPIN_MAX);
    }
    const float s0 = x32_sum(x16_sum(__uint_as_float(gu[0]))), s1 = x32_sum(x16_sum(__uint_as_float(gu[2])));
    const float g0 = rbf(__uint_as_float(__builtin_amdgcn_readlane(__float_as_uint(s0), w)));
    const float g1 = rbf(__uint_as_float(__builtin_amdgcn_readlane(__float_as_uint(s1), w)));
    const float u0 = rbf(__uint_as_float(__builtin_amdgcn_readlane(__float_as_uint(s0), 8 + w)));
    const float u1 = rbf(__uint_as_float(__builtin_amdgcn_readlane(__float_as_uint(s1), 8 + w)));
    const unsigned hp = pk_bf16(rbf(g0 / (1.f + __expf(-g0))) * u0, rbf(g1 / (1.f + __expf(-g1))) * u1);
    f32x4 z0, z1;
#pragma unroll
    for (int k = 0; k < 2; ++k) {
      z0[2 * k] = dot2(perm_lo(Fd[0][k], Fd[1][k]), hp, 0.f);
      z0[2 * k + 1] = dot2(perm_hi(Fd[0][k], Fd[1][k]), hp, 0.f);
      z1[2 * k] = dot2(perm_lo(Fd[0][k + 2], Fd[1][k + 2]), hp, 0.f);
      z1[2 * k + 1] = dot2(perm_hi(Fd[0][k + 2], Fd[1][k + 2]), hp, 0.f);
    }
    if (lane < 56) {
      *reinterpret_cast<f32x4*>(lds + L_YRED + w * 448 + lane * 8) = z0;
      *reinterpret_cast<f32x4*>(lds + L_YRED + w * 448 + lane * 8 + 4) = z1;
    }
    LDS_DRAIN();
  }
  vphase<16>(R, cx, vs, pl, rws, rwz, ml_off, z_off, SEQ(8));
  CFENCE();
  if (t < 224) {
    f32x2 zz = {0.f, 0.f};
#pragma unroll
    for (int ww = 0; ww < 8; ++ww) zz += *reinterpret_cast<const f32x2*>(lds + L_YRED + ww * 448 + 2 * t);
    const unsigned d0 = (__float_as_uint(zz[0]) + 0x80u) >> 8, d1 = (__float_as_uint(zz[1]) + 0x80u) >> 8;
    u32x4 gr = {d0 | g0tag, g1tag, d1 | g0tag, g1tag};
    __builtin_amdgcn_raw_buffer_store_b128(gr, rs, WS_Z + ((b * 56 + (t >> 2)) * 128 + zrow) * 64 + (t & 3) * 16, 0, 16);
  }
  vphase<24>(R, cx, vs, pl, rws, rwz, ml_off, z_off, SEQ(FSTEP - 24));
  static_assert(idx_of_kv(64 + FSTEP - 1) > WK + 64 + MLSTEP, "(m,l) gather landed");
  float fscale;
  bool mlok;
  {
    PIN1(vs.mlg);
    mlok = __all(vs.mlg[1] == tlo && vs.mlg[3] == tlo);
    const float mi = __uint_as_float(vs.mlg[0]), li = __uint_as_float(vs.mlg[2]);
    const float M = wave_max(mi);
    const float L = wave_sum(li * __expf(mi - M));
    fscale = __expf(mw - M) / L;
    PIN1(fscale);
  }
  vphase<FSTEP>(R, cx, vs, pl, rws, rwz, ml_off, z_off, SEQ(64 - FSTEP));
  asm volatile("s_waitcnt vmcnt(0)" : "+v"(vs.zg));
  STAMP(6);
  if (!mlok) {
    unsigned spins = 0;
    u32x4 mg;
    do {
      ld_sc1(mg, ml_off, rws);
      waitv<0>(mg);
    } while (!__all(mg[1] == tlo && mg[3] == tlo) && ++spins < SPIN_MAX);
    const float mi = __uint_as_float(mg[0]), li = __uint_as_float(mg[2]);
    const float M = wave_max(mi);
    const float L = wave_sum(li * __expf(mi - M));
    fscale = __expf(mw - M) / L;
  }
  {
    f32x4 oh;
#pragma unroll
    for (int k = 0; k < 4; ++k) oh[k] = sw32(vs.acc[k], vs.acc[k + 4]) * fscale;
    *reinterpret_cast<f32x4*>(lds + L_ORED + w * 256 + l32 * 8 + half * 4) = oh;
  }
  bar();
  {
    float ov = 0.f;
#pragma unroll
    for (int ww = 0; ww < 8; ++ww) ov += lds[L_ORED + ww * 256 + w * 32 + l32];
    const float on = dpp<XOR1>(ov);
    const unsigned opk = pk_bf16(ov, on);
    float ya[8];
#pragma unroll
    for (int k = 0; k < 8; ++k) ya[k] = 0.f;
    ophase(R, opk, ya, SEQ(16));
    if (lane < 56) {
      *reinterpret_cast<f32x4*>(lds + L_YRED + w * 448 + lane * 8) = f32x4{ya[0], ya[1], ya[2], ya[3]};
      *reinterpret_cast<f32x4*>(lds + L_YRED + w * 448 + lane * 8 + 4) = f32x4{ya[4], ya[5], ya[6], ya[7]};
    }
  }
  bar();
  if (t < 224) {
    f32x2 yy = {0.f, 0.f};
#pragma unroll
    for (int ww = 0; ww < 8; ++ww) yy += *reinterpret_cast<const f32x2*>(lds + L_YRED + ww * 448 + 2 * t);
    const unsigned d0 = (__float_as_uint(yy[0]) + 0x80u) >> 8, d1 = (__float_as_uint(yy[1]) + 0x80u) >> 8;
    u32x4 gr = {d0 | g0tag, g1tag, d1 | g0tag, g1tag};
    __builtin_amdgcn_raw_buffer_store_b128(gr, rs, WS_Y + ((b * 56 + (t >> 2)) * 128 + pidx) * 64 + (t & 3) * 16, 0, 16);
  }
  if (!is_cons) return;

  {
    const int y_off = WS_Y + ((b * 56 + ci) * 128) * 64 + t * 16;
    u32x4 yg;
    ld_sc1(yg, y_off, rws);
    const bool zlive = t < 448;
    u32x4 zg = vs.zg;
    unsigned spins = 0;
    for (;;) {
      const bool ok = !zlive || ((zg[0] & 0xff000000u) == g0tag && zg[1] == g1tag && (zg[2] & 0xff000000u) == g0tag && zg[3] == g1tag);
      if (__all(ok) || ++spins >= SPIN_MAX) break;
      __builtin_amdgcn_s_sleep(2);
      if (!ok) ld_sc1_re(zg, z_off, rws);
    }
    float z0 = zlive ? __uint_as_float(zg[0] << 8) : 0.f, z1 = zlive ? __uint_as_float(zg[2] << 8) : 0.f;
    z0 += dpp<ROR4>(z0); z0 += dpp<XOR8>(z0); z0 = x16_sum(z0); z0 = x32_sum(z0);
    z1 += dpp<ROR4>(z1); z1 += dpp<XOR8>(z1); z1 = x16_sum(z1); z1 = x32_sum(z1);
    PIN1(z0); PIN1(z1);
    waitv<0>(yg);
    spins = 0;
    for (;;) {
      const bool ok = (yg[0] & 0xff000000u) == g0tag && yg[1] == g1tag && (yg[2] & 0xff000000u) == g0tag && yg[3] == g1tag;
      if (__all(ok) || ++spins >= SPIN_MAX) break;
      __builtin_amdgcn_s_sleep(2);
      if (!ok) ld_sc1_re(yg, y_off, rws);
    }
    float y0 = __uint_as_float(yg[0] << 8), y1 = __uint_as_float(yg[2] << 8);
    y0 += dpp<ROR4>(y0); y0 += dpp<XOR8>(y0); y0 = x16_sum(y0); y0 = x32_sum(y0);
    y1 += dpp<ROR4>(y1); y1 += dpp<XOR8>(y1); y1 = x16_sum(y1); y1 = x32_sum(y1);
    if (lane < 4) {
      *reinterpret_cast<f32x2*>(lds + L_C + w * 16 + lane * 2) = f32x2{y0, y1};
      *reinterpret_cast<f32x2*>(lds + L_C + w * 16 + 8 + lane * 2) = f32x2{z0, z1};
    }
    bar();
    if (t < 8) {
      float a = 0.f, f = 0.f;
#pragma unroll
      for (int ww = 0; ww < 8; ++ww) { a += lds[L_C + ww * 16 + t]; f += lds[L_C + ww * 16 + 8 + t]; }
      const unsigned xo = reinterpret_cast<const u16*>(ldsu + L_X)[ci * 8 + t];
      const float xr = __uint_as_float(xo << 16);
      const float res = rbf(xr + rbf(a)) + rbf(f);
      out[b * D_ + ci * 8 + t] = __builtin_bit_cast(u16, (__bf16)res);
    }
    STAMP(11);
    if (ci == 0 && t == 0) {
      u32x2 ne = {tlo, thi_full};
      __builtin_amdgcn_raw_buffer_store_b64(ne, rs, WS_EPOCH + b * 256, 0, 16);
    }
  }
}

extern "C" void kernel_launch(void* const* d_in, const int* in_sizes, int n_in,
                              void* d_out, int out_size, void* d_ws, size_t ws_size,
                              hipStream_t stream) {
  const u16* x = (const u16*)d_in[0];
  const u16* k = (const u16*)d_in[1];
  const u16* v = (const u16*)d_in[2];
  const u16* wq = (const u16*)d_in[3];
  const u16* wo = (const u16*)d_in[4];
  const u16* wg = (const u16*)d_in[6];
  const u16* wu = (const u16*)d_in[7];
  const u16* wd = (const u16*)d_in[8];
  fused_kernel<<<NWG, NTHR, 0, stream>>>(x, k, v, wq, wo, wg, wu, wd, (u16*)d_out, (unsigned char*)d_ws);
}
```

```cpp
#include <hip/hip_runtime.h>
#include <hip/hip_bf16.h>
#include <stdint.h>
#include <utility>

typedef unsigned short u16;
typedef unsigned u32x4 __attribute__((ext_vector_type(4)));
typedef unsigned u32x2 __attribute__((ext_vector_type(2)));
typedef float f32x4 __attribute__((ext_vector_type(4)));
typedef float f32x2 __attribute__((ext_vector_type(2)));
typedef __bf16 bf16x2 __attribute__((ext_vector_type(2)));

constexpr int D_ = 448, H_ = 16, SK = 8192, DH = 256, F_ = 1792;
constexpr int NWG = 256, NTHR = 512;
constexpr int KEYS_WG = 1024;

constexpr int WK = 44;
constexpr int KA = 16;
constexpr int WOP = 3;
constexpr int NISS = 160;
#define GUSTEP 44
#define MLSTEP 40
constexpr int FSTEP = 60;
constexpr int nwo_before(int c) { return c / WOP < 32 ? c / WOP : 32; }
constexpr bool is_wo_step(int c) { return (c % WOP) == WOP - 1 && c / WOP < 32; }
constexpr int kv_of_step(int c) { return WK + c - nwo_before(c); }
constexpr int idx_of_kv(int m) {
  if (m < WK) return m;
  for (int c = 0; c < NISS; ++c) if (!is_wo_step(c) && kv_of_step(c) == m) return WK + c;
  return -1;
}
constexpr int idx_of_wo(int k) { return WK + k * WOP + WOP - 1; }
constexpr int slot_of(int n) { return n < WK ? n : slot_of(idx_of_kv(n - WK)); }
constexpr int wait_of_step(int c) { return ((WK + c < NISS) ? WK + c : NISS) - idx_of_kv(c) - 1; }
static_assert(idx_of_kv(127) < NISS && idx_of_kv(127) > 0 && idx_of_wo(31) < NISS && WK > 32 && KA <= 16, "plan");

constexpr int WS_EPOCH = 0;
constexpr int WS_ML = 4096;
constexpr int WS_Q = WS_ML + 2 * 16 * 64 * 16;
constexpr int WS_Y = WS_Q + 2 * 16 * 4096;
constexpr int WS_Z = WS_Y + 2 * 56 * 128 * 64;
constexpr int WS_GU = WS_Z + 2 * 56 * 128 * 64;
constexpr int WS_END = WS_GU + 2 * 128 * 1024;

constexpr int L_XN = 0;
constexpr int L_ORED = L_XN + 5 * 256;
constexpr int L_FRED = L_ORED + 8 * 256;
constexpr int L_YRED = L_FRED + 8 * 128;
constexpr int L_P = L_YRED + 8 * 448;
constexpr int L_C = L_P + 8 * 128;
constexpr int L_X = L_C + 8 * 16;
constexpr int L_Q = L_X + 256;
constexpr int L_CNT = L_Q + 128;
constexpr int L_QP = L_CNT + 4;
constexpr int L_END = L_QP + 256;

constexpr unsigned SPIN_MAX = 1u << 18;

__device__ __forceinline__ float dot2(unsigned a, unsigned b, float c) {
  return __builtin_amdgcn_fdot2_f32_bf16(__builtin_bit_cast(bf16x2, a), __builtin_bit_cast(bf16x2, b), c, false);
}
__device__ __forceinline__ float bflo(unsigned v) { return __uint_as_float(v << 16); }
__device__ __forceinline__ float bfhi(unsigned v) { return __uint_as_float(v & 0xffff0000u); }
__device__ __forceinline__ unsigned pk_bf16(float lo, float hi) {
  bf16x2 r; r[0] = (__bf16)lo; r[1] = (__bf16)hi; return __builtin_bit_cast(unsigned, r);
}
__device__ __forceinline__ float rbf(float x) { return (float)(__bf16)x; }
__device__ __forceinline__ unsigned perm_lo(unsigned a, unsigned b) { return __builtin_amdgcn_perm(b, a, 0x05040100u); }
__device__ __forceinline__ unsigned perm_hi(unsigned a, unsigned b) { return __builtin_amdgcn_perm(b, a, 0x07060302u); }
template <int CTRL> __device__ __forceinline__ float dpp(float x) {
  return __builtin_bit_cast(float, __builtin_amdgcn_mov_dpp(__builtin_bit_cast(int, x), CTRL, 0xf, 0xf, true));
}
constexpr int XOR1 = 0xB1, XOR2 = 0x4E, XOR7 = 0x141, XOR8 = 0x128, ROR4 = 0x124;
__device__ __forceinline__ float sw16(float a, float b) {
  auto s = __builtin_amdgcn_permlane16_swap(__float_as_uint(a), __float_as_uint(b), false, false);
  return __uint_as_float(s[0]) + __uint_as_float(s[1]);
}
__device__ __forceinline__ float sw32(float a, float b) {
  auto s = __builtin_amdgcn_permlane32_swap(__float_as_uint(a), __float_as_uint(b), false, false);
  return __uint_as_float(s[0]) + __uint_as_float(s[1]);
}
__device__ __forceinline__ float x16_sum(float x) { return sw16(x, x); }
__device__ __forceinline__ float x32_sum(float x) { return sw32(x, x); }
__device__ __forceinline__ float x16_max(float x) {
  auto s = __builtin_amdgcn_permlane16_swap(__float_as_uint(x), __float_as_uint(x), false, false);
  return fmaxf(__uint_as_float(s[0]), __uint_as_float(s[1]));
}
__device__ __forceinline__ float x32_max(float x) {
  auto s = __builtin_amdgcn_permlane32_swap(__float_as_uint(x), __float_as_uint(x), false, false);
  return fmaxf(__uint_as_float(s[0]), __uint_as_float(s[1]));
}
__device__ __forceinline__ float wave_sum(float x) {
  x += dpp<XOR1>(x); x += dpp<XOR2>(x); x += dpp<XOR7>(x); x += dpp<XOR8>(x);
  x = x16_sum(x); x = x32_sum(x); return x;
}
__device__ __forceinline__ float wave_max(float x) {
  x = fmaxf(x, dpp<XOR1>(x)); x = fmaxf(x, dpp<XOR2>(x)); x = fmaxf(x, dpp<XOR7>(x)); x = fmaxf(x, dpp<XOR8>(x));
  x = x16_max(x); x = x32_max(x); return x;
}
template <int CTRL, int BIT> __device__ __forceinline__ float trs(float lo, float hi, int lane) {
  const float t1 = lo + dpp<CTRL>(lo), t2 = hi + dpp<CTRL>(hi);
  return (lane & BIT) ? t2 : t1;
}
__device__ __forceinline__ void bar() { asm volatile("s_waitcnt lgkmcnt(0)\n\ts_barrier" ::: "memory"); }
#define PIN8(a_) asm volatile("" : "+v"(a_[0]), "+v"(a_[1]), "+v"(a_[2]), "+v"(a_[3]), "+v"(a_[4]), "+v"(a_[5]), "+v"(a_[6]), "+v"(a_[7]))
#define PIN1(a_) asm volatile("" : "+v"(a_))
#define CFENCE() asm volatile("" ::: "memory")
#define LDS_DRAIN() asm volatile("s_waitcnt lgkmcnt(0)" ::: "memory")

__device__ __forceinline__ u32x4 mkdesc(const void* p, unsigned bytes) {
  const unsigned long long a = (unsigned long long)p;
  u32x4 d;
  d[0] = __builtin_amdgcn_readfirstlane((unsigned)a);
  d[1] = __builtin_amdgcn_readfirstlane((unsigned)(a >> 32) & 0xffffu);
  d[2] = __builtin_amdgcn_readfirstlane(bytes);
  d[3] = 0x00020000u;
  return d;
}
template <int OFF> __device__ __forceinline__ void ld_nt_new(u32x4& d, int voff, const u32x4& rsrc, int soff) {
  asm volatile("buffer_load_dwordx4 %0, %1, %2, %3 offen offset:%4 nt" : "=v"(d) : "v"(voff), "s"(rsrc), "s"(soff), "i"(OFF));
}
template <int OFF> __device__ __forceinline__ void ld_nt_re(u32x4& d, int voff, const u32x4& rsrc, int soff) {
  asm volatile("buffer_load_dwordx4 %0, %1, %2, %3 offen offset:%4 nt" : "+v"(d) : "v"(voff), "s"(rsrc), "s"(soff), "i"(OFF));
}
template <int OFF> __device__ __forceinline__ void ld_pl_re(u32x4& d, int voff, const u32x4& rsrc, int soff) {
  asm volatile("buffer_load_dwordx4 %0, %1, %2, %3 offen offset:%4" : "+v"(d) : "v"(voff), "s"(rsrc), "s"(soff), "i"(OFF));
}
template <int OFF> __device__ __forceinline__ void ld_w(u32x4& d, int voff, const u32x4& rsrc) {
  asm volatile("buffer_load_dwordx4 %0, %1, %2, 0 offen offset:%3" : "=v"(d) : "v"(voff), "s"(rsrc), "i"(OFF));
}
__device__ __forceinline__ void ld_ws(u32x4& d, int voff, const u32x4& rsrc, int soff) {
  asm volatile("buffer_load_dwordx4 %0, %1, %2, %3 offen" : "=v"(d) : "v"(voff), "s"(rsrc), "s"(soff));
}
__device__ __forceinline__ void ld_sc1(u32x4& d, int voff, const u32x4& rsrc) {
  asm volatile("buffer_load_dwordx4 %0, %1, %2, 0 offen sc1" : "=v"(d) : "v"(voff), "s"(rsrc) : "memory");
}
__device__ __forceinline__ void ld_sc1_re(u32x4& d, int voff, const u32x4& rsrc) {
  asm volatile("buffer_load_dwordx4 %0, %1, %2, 0 offen sc1\n\ts_waitcnt vmcnt(0)" : "+v"(d) : "v"(voff), "s"(rsrc) : "memory");
}
template <int N> __device__ __forceinline__ void waitv(u32x4& d) { asm volatile("s_waitcnt vmcnt(%1)" : "+v"(d) : "i"(N)); }

struct Ctx {
  u32x4 rk, rv, ro;
  int lo16, wvo;
};
template <int n> __device__ __forceinline__ void issue(u32x4 (&R)[WK], const Ctx& c) {
  if constexpr (n < NISS) {
    constexpr int sl = slot_of(n);
    if constexpr (n < WK) {
      if constexpr (n >= KA) ld_nt_new<(n & 3) * 1024>(R[sl], c.lo16, c.rk, (n >> 2) * 32768);
      else ld_nt_re<(n & 3) * 1024>(R[sl], c.lo16, c.rk, (n >> 2) * 32768);
    } else if constexpr (is_wo_step(n - WK)) {
      constexpr int k = nwo_before(n - WK);
      ld_pl_re<(k & 3) * 896>(R[sl], c.wvo, c.ro, (k >> 2) * 3584);
    } else {
      constexpr int m = kv_of_step(n - WK);
      if constexpr (m < 64) ld_nt_re<(m & 3) * 1024>(R[sl], c.lo16, c.rk, (m >> 2) * 32768);
      else ld_nt_re<((m - 64) & 3) * 1024>(R[sl], c.lo16, c.rv, ((m - 64) >> 2) * 32768);
    }
  }
}
template <int A, int... I> __device__ __forceinline__ void issue_range(u32x4 (&R)[WK], const Ctx& c, std::integer_sequence<int, I...>) {
  (issue<A + I>(R, c), ...);
}

struct KState { float t0, t1, t2, t3, t4; float sc[2]; };
template <int c> __device__ __forceinline__ void kstep(u32x4 (&R)[WK], const Ctx& cx, const u32x4& q4, KState& st, int lane) {
  constexpr int sl = slot_of(idx_of_kv(c));
  waitv<wait_of_step(c)>(R[sl]);
  float d = dot2(R[sl][0], q4[0], 0.f);
  d = dot2(R[sl][1], q4[1], d);
  d = dot2(R[sl][2], q4[2], d);
  d = dot2(R[sl][3], q4[3], d);
  PIN1(d);
  issue<WK + c>(R, cx);
  if constexpr ((c & 7) == 7) __builtin_amdgcn_s_barrier();
  constexpr int i = c & 31;
  if constexpr ((i & 1) == 0) { st.t0 = d; return; }
  else {
    d = sw16(st.t0, d);
    if constexpr ((i & 2) == 0) { st.t1 = d; return; }
    else {
      d = trs<XOR8, 8>(st.t1, d, lane);
      if constexpr ((i & 4) == 0) { st.t2 = d; return; }
      else {
        d = trs<XOR7, 4>(st.t2, d, lane);
        if constexpr ((i & 8) == 0) { st.t3 = d; return; }
        else {
          d = trs<XOR2, 2>(st.t3, d, lane);
          if constexpr ((i & 16) == 0) { st.t4 = d; return; }
          else {
            d = trs<XOR1, 1>(st.t4, d, lane);
            st.sc[c >> 5] = rbf(d) * 0.0625f;
          }
        }
      }
    }
  }
}
template <int A, int... I> __device__ __forceinline__ void kphase(u32x4 (&R)[WK], const Ctx& cx, const u32x4& q4, KState& st, int lane, std::integer_sequence<int, I...>) {
  (kstep<A + I>(R, cx, q4, st, lane), ...);
}

struct VState { float acc[8]; f32x4 p4; u32x4 mlg, zg; };
template <int j> __device__ __forceinline__ void vstep(u32x4 (&R)[WK], const Ctx& cx, VState& st, const float* pl, const u32x4& rws, const u32x4& rwz, int ml_off, int z_off) {
  constexpr int c = 64 + j;
  constexpr int sl = slot_of(idx_of_kv(c));
  if constexpr ((j & 3) == 0) st.p4 = *reinterpret_cast<const f32x4*>(pl + j);
  waitv<wait_of_step(c)>(R[sl]);
  const float p = st.p4[j & 3];
#pragma unroll
  for (int k = 0; k < 4; ++k) {
    st.acc[2 * k] = fmaf(p, bflo(R[sl][k]), st.acc[2 * k]);
    st.acc[2 * k + 1] = fmaf(p, bfhi(R[sl][k]), st.acc[2 * k + 1]);
  }
  PIN8(st.acc);
  issue<WK + c>(R, cx);
  if constexpr (j == MLSTEP) { ld_sc1(st.mlg, ml_off, rws); ld_sc1(st.zg, z_off, rwz); }
  if constexpr (j >= 48) {
    constexpr int i = j - 48;
    static_assert(idx_of_kv(64 + j) > idx_of_wo(2 * i + 1), "wo pair landed");
    constexpr int sa = slot_of(idx_of_wo(2 * i)), sb = slot_of(idx_of_wo(2 * i + 1));
    PIN1(R[sa]); PIN1(R[sb]);
#pragma unroll
    for (int k = 0; k < 4; ++k) {
      const unsigned a = R[sa][k], e = R[sb][k];
      R[sa][k] = perm_lo(a, e); R[sb][k] = perm_hi(a, e);
    }
    PIN1(R[sa]); PIN1(R[sb]);
  }
  if constexpr ((j & 7) == 7 && j < 48) __builtin_amdgcn_s_barrier();
}
template <int A, int... I> __device__ __forceinline__ void vphase(u32x4 (&R)[WK], const Ctx& cx, VState& st, const float* pl, const u32x4& rws, const u32x4& rwz, int ml_off, int z_off, std::integer_sequence<int, I...>) {
  (vstep<A + I>(R, cx, st, pl, rws, rwz, ml_off, z_off), ...);
}
template <int i> __device__ __forceinline__ void ostep(u32x4 (&R)[WK], unsigned opk, float (&ya)[8]) {
  constexpr int sa = slot_of(idx_of_wo(2 * i)), sb = slot_of(idx_of_wo(2 * i + 1));
  const unsigned so = __builtin_amdgcn_readlane(opk, 2 * i);
#pragma unroll
  for (int k = 0; k < 4; ++k) {
    ya[2 * k] = dot2(R[sa][k], so, ya[2 * k]);
    ya[2 * k + 1] = dot2(R[sb][k], so, ya[2 * k + 1]);
  }
}
template <int... I> __device__ __forceinline__ void ophase(u32x4 (&R)[WK], unsigned opk, float (&ya)[8], std::integer_sequence<int, I...>) {
  (ostep<I>(R, opk, ya), ...);
}
#define SEQ(n_) std::make_integer_sequence<int, (n_)>{}

__global__ void __launch_bounds__(NTHR, 2)
fused_kernel(const u16* __restrict__ x, const u16* __restrict__ kc, const u16* __restrict__ vc,
             const u16* __restrict__ wq, const u16* __restrict__ wo, const u16* __restrict__ wg,
             const u16* __restrict__ wu, const u16* __restrict__ wd, u16* __restrict__ out,
             unsigned char* ws) {
  __shared__ __attribute__((aligned(16))) float lds[L_END];
  unsigned* ldsu = reinterpret_cast<unsigned*>(lds);
  const int t = threadIdx.x, lane = t & 63;
  const int w = __builtin_amdgcn_readfirstlane(t >> 6);
  const int g = blockIdx.x;
  const int xcd = g & 7, r = g >> 3, hsel = (r >> 3) & 1, b = r >> 4, s = r & 7;
  const int h = xcd + 8 * hsel;
  const int i16 = hsel * 8 + s;
  const int pidx = h * 8 + s;
  const bool has_ffn = i16 < 14;
  const int slice = xcd * 14 + (has_ffn ? i16 : 13);
  const int zrow = has_ffn ? slice : (112 + xcd * 2 + (i16 - 14));
  const bool is_cons = i16 >= 9;
  const int ci = xcd * 7 + (is_cons ? (i16 - 9) : 0);
  const int half = lane >> 5, l32 = lane & 31;
  const int l56 = lane < 56 ? lane : lane - 8;

  const __amdgpu_buffer_rsrc_t rs = __builtin_amdgcn_make_buffer_rsrc((void*)ws, 0, WS_END, 0x00020000);
  const u32x4 rws = mkdesc(ws, WS_END);
  const size_t kvoff = ((size_t)(b * H_ + h) * SK + s * KEYS_WG + w * 8) * DH;
  Ctx cx;
  cx.rk = mkdesc(kc + kvoff, 524288 - w * 4096);
  cx.rv = mkdesc(vc + kvoff, 524288 - w * 4096);
  cx.ro = mkdesc(wo + (size_t)(h * 256 + w * 32) * D_, 32 * 896);
  cx.lo16 = lane * 16;
  cx.wvo = l56 * 16;
  u32x4 R[WK];
#pragma unroll
  for (int i = 0; i < KA; ++i) asm volatile("" : "=v"(R[i]));
  if (t == 0) ldsu[L_CNT] = 0u;
  bar();
  u32x4 ep;
  asm volatile("s_nop 4");
  ld_sc1(ep, WS_EPOCH + b * 256, rws);
  u32x4 xv, Fq[8];
  const bool worker = (w >= 1 && w <= 4);
  const int qline = xcd * 8 + (i16 >> 1), qrh = i16 & 1;
  const int j0 = ((t - 64) >> 3) & 31;
  if (w == 0) {
    issue_range<0>(R, cx, SEQ(KA));
  } else if (worker) {
    const u32x4 rx = mkdesc(x + b * D_, D_ * 2);
    const u32x4 rq = mkdesc(wq + (size_t)(qrh * 224) * 4096 + qline * 64, 223 * 8192 + 128);
    const int qvo = j0 * 16384 + (lane & 7) * 16;
    asm volatile("s_nop 4");
    ld_ws(xv, cx.wvo, rx, 0);
#pragma unroll
    for (int i = 0; i < 4; ++i) {
      ld_ws(Fq[2 * i], qvo, rq, i * 524288);
      ld_ws(Fq[2 * i + 1], qvo, rq, i * 524288 + 8192);
    }
  }
  CFENCE();
  if (w == 0) {
    waitv<KA>(ep);
    const unsigned tl0 = __builtin_amdgcn_readfirstlane(ep[0]) + 1u;
    {
      unsigned spins = 0;
      while (__hip_atomic_load(ldsu + L_CNT, __ATOMIC_RELAXED, __HIP_MEMORY_SCOPE_WORKGROUP) != 4u && ++spins < (SPIN_MAX << 4)) __builtin_amdgcn_s_sleep(1);
      CFENCE();
    }
    __builtin_amdgcn_s_setprio(3);
    if (lane < 32) {
      f32x2 a = {0.f, 0.f};
#pragma unroll
      for (int ww = 0; ww < 4; ++ww) a += *reinterpret_cast<const f32x2*>(lds + L_QP + ww * 64 + lane * 2);
      u32x4 gr = {__float_as_uint(a[0]), tl0, __float_as_uint(a[1]), tl0};
      __builtin_amdgcn_raw_buffer_store_b128(gr, rs, WS_Q + ((b * H_ + (qline >> 2)) * 2 + qrh) * 2048 + ((qline & 3) * 64 + lane * 2) * 8, 0, 16);
    }
    const int q_off = WS_Q + (b * H_ + h) * 4096 + lane * 32;
    u32x4 qa0, qa1, qb0, qb1;
    unsigned spins = 0;
    bool okq;
    do {
      ld_sc1(qa0, q_off, rws); ld_sc1(qa1, q_off + 16, rws); ld_sc1(qb0, q_off + 2048, rws); ld_sc1(qb1, q_off + 2064, rws);
      asm volatile("s_waitcnt vmcnt(0)" : "+v"(qa0), "+v"(qa1), "+v"(qb0), "+v"(qb1));
      okq = qa0[1] == tl0 && qa0[3] == tl0 && qa1[1] == tl0 && qa1[3] == tl0 && qb0[1] == tl0 && qb0[3] == tl0 && qb1[1] == tl0 && qb1[3] == tl0;
    } while (!__all(okq) && ++spins < SPIN_MAX);
    *reinterpret_cast<u32x2*>(ldsu + L_Q + lane * 2) = u32x2{
        pk_bf16(__uint_as_float(qa0[0]) + __uint_as_float(qb0[0]), __uint_as_float(qa0[2]) + __uint_as_float(qb0[2])),
        pk_bf16(__uint_as_float(qa1[0]) + __uint_as_float(qb1[0]), __uint_as_float(qa1[2]) + __uint_as_float(qb1[2]))};
    __builtin_amdgcn_s_setprio(0);
  } else if (worker) {
    waitv<8>(xv);
    {
      float xf[8];
#pragma unroll
      for (int k = 0; k < 4; ++k) { xf[2 * k] = bflo(xv[k]); xf[2 * k + 1] = bfhi(xv[k]); }
      float sm = 0.f;
#pragma unroll
      for (int k = 0; k < 8; ++k) sm += xf[k];
      sm = lane < 56 ? sm : 0.f;
      sm = wave_sum(sm);
      const float mexact = sm / (float)D_;
      const float m = rbf(mexact);
      float vs = 0.f;
#pragma unroll
      for (int k = 0; k < 8; ++k) { float d = xf[k] - mexact; vs += d * d; }
      vs = lane < 56 ? vs : 0.f;
      vs = wave_sum(vs);
      const float var = rbf(vs / (float)D_);
      const float den = rbf(sqrtf(rbf(var + 1e-5f)));
      u32x4 xn;
#pragma unroll
      for (int k = 0; k < 4; ++k) xn[k] = pk_bf16(rbf(xf[2 * k] - m) / den, rbf(xf[2 * k + 1] - m) / den);
      if (lane < 56) {
        *reinterpret_cast<u32x4*>(ldsu + L_XN + w * 256 + lane * 4) = xn;
        if (w == 1) *reinterpret_cast<u32x4*>(ldsu + L_X + lane * 4) = xv;
      }
    }
    unsigned xp[4];
#pragma unroll
    for (int i = 0; i < 3; ++i) xp[i] = ldsu[L_XN + w * 256 + qrh * 112 + j0 + 32 * i];
    xp[3] = ldsu[L_XN + w * 256 + qrh * 112 + (j0 < 16 ? j0 + 96 : j0)];
    xp[3] = (j0 < 16) ? xp[3] : 0u;
    asm volatile("s_waitcnt vmcnt(0)" : "+v"(Fq[0]), "+v"(Fq[1]), "+v"(Fq[2]), "+v"(Fq[3]), "+v"(Fq[4]), "+v"(Fq[5]), "+v"(Fq[6]), "+v"(Fq[7]));
    {
      float fa[8];
#pragma unroll
      for (int k = 0; k < 8; ++k) fa[k] = 0.f;
#pragma unroll
      for (int i = 0; i < 4; ++i)
#pragma unroll
        for (int k = 0; k < 4; ++k) {
          fa[2 * k] = dot2(perm_lo(Fq[2 * i][k], Fq[2 * i + 1][k]), xp[i], fa[2 * k]);
          fa[2 * k + 1] = dot2(perm_hi(Fq[2 * i][k], Fq[2 * i + 1][k]), xp[i], fa[2 * k + 1]);
        }
      float f4[4], f2[2];
#pragma unroll
      for (int k = 0; k < 4; ++k) f4[k] = sw32(fa[k], fa[k + 4]);
#pragma unroll
      for (int k = 0; k < 2; ++k) f2[k] = sw16(f4[k], f4[k + 2]);
      const float f1 = trs<XOR8, 8>(f2[0], f2[1], lane);
      lds[L_QP + (w - 1) * 64 + (lane & 7) * 8 + (lane >> 5) * 4 + ((lane >> 4) & 1) * 2 + ((lane >> 3) & 1)] = f1;
    }
    LDS_DRAIN();
    if (lane == 0) __hip_atomic_fetch_add(ldsu + L_CNT, 1u, __ATOMIC_RELAXED, __HIP_MEMORY_SCOPE_WORKGROUP);
  }
  if (w != 0) issue_range<0>(R, cx, SEQ(KA));
  CFENCE();
  if (w == 0) bar();
  u32x4 Fg[4], Fd[2];
  {
    const u32x4 rg = mkdesc(wg + (size_t)((slice & 3) * 112) * F_ + (slice >> 2) * 64, 111 * F_ * 2 + 128);
    const u32x4 ru = mkdesc(wu + (size_t)((slice & 3) * 112) * F_ + (slice >> 2) * 64, 111 * F_ * 2 + 128);
    const u32x4 rd = mkdesc(wd + (size_t)(slice * 16 + 2 * w) * D_, 2 * 896);
    const int fvo = (t >> 3) * (2 * F_ * 2) + (t & 7) * 16;
    asm volatile("s_nop 4");
    ld_w<0>(Fg[0], fvo, rg); ld_w<F_ * 2>(Fg[1], fvo, rg);
    ld_w<0>(Fg[2], fvo, ru); ld_w<F_ * 2>(Fg[3], fvo, ru);
    ld_w<0>(Fd[0], cx.wvo, rd); ld_w<896>(Fd[1], cx.wvo, rd);
  }
  issue_range<KA>(R, cx, SEQ(WK - KA));
  if (w != 0) bar();
  waitv<WK - KA>(ep);
  const unsigned long long e64 = (((unsigned long long)ep[1] << 32) | ep[0]) + 1ull;
  const unsigned tlo = __builtin_amdgcn_readfirstlane((unsigned)e64);
  const unsigned thi_full = __builtin_amdgcn_readfirstlane((unsigned)(e64 >> 32));
  const unsigned g0tag = tlo << 24, g1tag = (tlo >> 8) | ((thi_full & 0xffu) << 24);
  const u32x4 q4 = *reinterpret_cast<const u32x4*>(ldsu + L_Q + l32 * 4);

  KState ks;
  kphase<0>(R, cx, q4, ks, lane, SEQ(24));
  {
    asm volatile("" : "+v"(Fg[0]), "+v"(Fg[1]), "+v"(Fg[2]), "+v"(Fg[3]), "+v"(Fd[0]), "+v"(Fd[1]));
    const int jj = t >> 3;
    const unsigned xa = ldsu[L_XN + 256 + (slice & 3) * 56 + (jj < 56 ? jj : 0)];
    float fa[16];
#pragma unroll
    for (int k = 0; k < 4; ++k) {
      fa[2 * k] = dot2(perm_lo(Fg[0][k], Fg[1][k]), xa, 0.f);
      fa[2 * k + 1] = dot2(perm_hi(Fg[0][k], Fg[1][k]), xa, 0.f);
      fa[8 + 2 * k] = dot2(perm_lo(Fg[2][k], Fg[3][k]), xa, 0.f);
      fa[8 + 2 * k + 1] = dot2(perm_hi(Fg[2][k], Fg[3][k]), xa, 0.f);
    }
    float f8[8], f4[4];
    f32x2 f2;
#pragma unroll
    for (int k = 0; k < 8; ++k) f8[k] = sw32(fa[k], fa[k + 8]);
#pragma unroll
    for (int k = 0; k < 4; ++k) f4[k] = sw16(f8[k], f8[k + 4]);
#pragma unroll
    for (int k = 0; k < 2; ++k) f2[k] = trs<XOR8, 8>(f4[k], f4[k + 2], lane);
    *reinterpret_cast<f32x2*>(lds + L_FRED + w * 128 + (lane >> 5) * 64 + (lane & 7) * 8 + ((lane >> 4) & 1) * 4 + ((lane >> 3) & 1) * 2) = f2;
    LDS_DRAIN();
  }
  kphase<24>(R, cx, q4, ks, lane, SEQ(8));
  CFENCE();
  if (w == 7) {
    f32x2 a = {0.f, 0.f};
#pragma unroll
    for (int ww = 0; ww < 7; ++ww) a += *reinterpret_cast<const f32x2*>(lds + L_FRED + ww * 128 + lane * 2);
    u32x4 gr = {__float_as_uint(a[0]), tlo, __float_as_uint(a[1]), tlo};
    __builtin_amdgcn_raw_buffer_store_b128(gr, rs, WS_GU + (b * 128 + zrow) * 1024 + lane * 16, 0, 16);
  }
  kphase<32>(R, cx, q4, ks, lane, SEQ(GUSTEP - 32));
  u32x4 gu;
  const int gu_off = WS_GU + (b * 128 + (slice & ~3) + (lane >> 4)) * 1024 + (((lane >> 3) & 1) * 64 + (slice & 3) * 16 + (lane & 7) * 2) * 8;
  ld_sc1(gu, gu_off, rws);
  kphase<GUSTEP>(R, cx, q4, ks, lane, SEQ(64 - GUSTEP));

  const float mw = wave_max(fmaxf(ks.sc[0], ks.sc[1]));
  {
    const float p0 = __expf(ks.sc[0] - mw), p1 = __expf(ks.sc[1] - mw);
    const float lw = wave_sum(p0 + p1);
    const int li = ((lane >> 4) & 1) | ((lane >> 2) & 2) | (lane & 4) | ((lane << 2) & 8) | ((lane << 4) & 16);
    lds[L_P + w * 128 + half * 64 + li] = p0;
    lds[L_P + w * 128 + half * 64 + 32 + li] = p1;
    if (lane == 0) {
      u32x4 gr = {__float_as_uint(mw), tlo, __float_as_uint(lw), tlo};
      __builtin_amdgcn_raw_buffer_store_b128(gr, rs, WS_ML + ((b * H_ + h) * 64 + s * 8 + w) * 16, 0, 16);
    }
  }

  VState vs;
#pragma unroll
  for (int k = 0; k < 8; ++k) vs.acc[k] = 0.f;
  const u32x4 rwz = mkdesc(ws, is_cons ? WS_END : 0);
  const int ml_off = WS_ML + ((b * H_ + h) * 64 + lane) * 16;
  const int z_off = WS_Z + ((b * 56 + ci) * 128) * 64 + t * 16;
  const float* pl = lds + L_P + w * 128 + half * 64;
  vphase<0>(R, cx, vs, pl, rws, rwz, ml_off, z_off, SEQ(16));
  static_assert(idx_of_kv(64 + 15) >= WK + GUSTEP, "gate/up gather landed");
  {
    PIN1(gu);
    if (!__all(gu[1] == tlo && gu[3] == tlo)) {
      unsigned spins = 0;
      do {
        ld_sc1(gu, gu_off, rws);
        waitv<0>(gu);
      } while (!__all(gu[1] == tlo && gu[3] == tlo) && ++spins < SPIN_MAX);
    }
    const float s0 = x32_sum(x16_sum(__uint_as_float(gu[0]))), s1 = x32_sum(x16_sum(__uint_as_float(gu[2])));
    const float g0 = rbf(__uint_as_float(__builtin_amdgcn_readlane(__float_as_uint(s0), w)));
    const float g1 = rbf(__uint_as_float(__builtin_amdgcn_readlane(__float_as_uint(s1), w)));
    const float u0 = rbf(__uint_as_float(__builtin_amdgcn_readlane(__float_as_uint(s0), 8 + w)));
    const float u1 = rbf(__uint_as_float(__builtin_amdgcn_readlane(__float_as_uint(s1), 8 + w)));
    const unsigned hp = pk_bf16(rbf(g0 / (1.f + __expf(-g0))) * u0, rbf(g1 / (1.f + __expf(-g1))) * u1);
    f32x4 z0, z1;
#pragma unroll
    for (int k = 0; k < 2; ++k) {
      z0[2 * k] = dot2(perm_lo(Fd[0][k], Fd[1][k]), hp, 0.f);
      z0[2 * k + 1] = dot2(perm_hi(Fd[0][k], Fd[1][k]), hp, 0.f);
      z1[2 * k] = dot2(perm_lo(Fd[0][k + 2], Fd[1][k + 2]), hp, 0.f);
      z1[2 * k + 1] = dot2(perm_hi(Fd[0][k + 2], Fd[1][k + 2]), hp, 0.f);
    }
    if (lane < 56) {
      *reinterpret_cast<f32x4*>(lds + L_YRED + w * 448 + lane * 8) = z0;
      *reinterpret_cast<f32x4*>(lds + L_YRED + w * 448 + lane * 8 + 4) = z1;
    }
    LDS_DRAIN();
  }
  vphase<16>(R, cx, vs, pl, rws, rwz, ml_off, z_off, SEQ(8));
  CFENCE();
  if (t < 224) {
    f32x2 zz = {0.f, 0.f};
#pragma unroll
    for (int ww = 0; ww < 8; ++ww) zz += *reinterpret_cast<const f32x2*>(lds + L_YRED + ww * 448 + 2 * t);
    const unsigned d0 = (__float_as_uint(zz[0]) + 0x80u) >> 8, d1 = (__float_as_uint(zz[1]) + 0x80u) >> 8;
    u32x4 gr = {d0 | g0tag, g1tag, d1 | g0tag, g1tag};
    __builtin_amdgcn_raw_buffer_store_b128(gr, rs, WS_Z + ((b * 56 + (t >> 2)) * 128 + zrow) * 64 + (t & 3) * 16, 0, 16);
  }
  vphase<24>(R, cx, vs, pl, rws, rwz, ml_off, z_off, SEQ(FSTEP - 24));
  static_assert(idx_of_kv(64 + FSTEP - 1) > WK + 64 + MLSTEP, "(m,l) gather landed");
  float fscale;
  bool mlok;
  {
    PIN1(vs.mlg);
    mlok = __all(vs.mlg[1] == tlo && vs.mlg[3] == tlo);
    const float mi = __uint_as_float(vs.mlg[0]), li = __uint_as_float(vs.mlg[2]);
    const float M = wave_max(mi);
    const float L = wave_sum(li * __expf(mi - M));
    fscale = __expf(mw - M) / L;
    PIN1(fscale);
  }
  vphase<FSTEP>(R, cx, vs, pl, rws, rwz, ml_off, z_off, SEQ(64 - FSTEP));
  asm volatile("s_waitcnt vmcnt(0)" : "+v"(vs.zg));
  if (!mlok) {
    unsigned spins = 0;
    u32x4 mg;
    do {
      ld_sc1(mg, ml_off, rws);
      waitv<0>(mg);
    } while (!__all(mg[1] == tlo && mg[3] == tlo) && ++spins < SPIN_MAX);
    const float mi = __uint_as_float(mg[0]), li = __uint_as_float(mg[2]);
    const float M = wave_max(mi);
    const float L = wave_sum(li * __expf(mi - M));
    fscale = __expf(mw - M) / L;
  }
  {
    f32x4 oh;
#pragma unroll
    for (int k = 0; k < 4; ++k) oh[k] = sw32(vs.acc[k], vs.acc[k + 4]) * fscale;
    *reinterpret_cast<f32x4*>(lds + L_ORED + w * 256 + l32 * 8 + half * 4) = oh;
  }
  bar();
  {
    float ov = 0.f;
#pragma unroll
    for (int ww = 0; ww < 8; ++ww) ov += lds[L_ORED + ww * 256 + w * 32 + l32];
    const float on = dpp<XOR1>(ov);
    const unsigned opk = pk_bf16(ov, on);
    float ya[8];
#pragma unroll
    for (int k = 0; k < 8; ++k) ya[k] = 0.f;
    ophase(R, opk, ya, SEQ(16));
    if (lane < 56) {
      *reinterpret_cast<f32x4*>(lds + L_YRED + w * 448 + lane * 8) = f32x4{ya[0], ya[1], ya[2], ya[3]};
      *reinterpret_cast<f32x4*>(lds + L_YRED + w * 448 + lane * 8 + 4) = f32x4{ya[4], ya[5], ya[6], ya[7]};
    }
  }
  bar();
  if (t < 224) {
    f32x2 yy = {0.f, 0.f};
#pragma unroll
    for (int ww = 0; ww < 8; ++ww) yy += *reinterpret_cast<const f32x2*>(lds + L_YRED + ww * 448 + 2 * t);
    const unsigned d0 = (__float_as_uint(yy[0]) + 0x80u) >> 8, d1 = (__float_as_uint(yy[1]) + 0x80u) >> 8;
    u32x4 gr = {d0 | g0tag, g1tag, d1 | g0tag, g1tag};
    __builtin_amdgcn_raw_buffer_store_b128(gr, rs, WS_Y + ((b * 56 + (t >> 2)) * 128 + pidx) * 64 + (t & 3) * 16, 0, 16);
  }
  if (!is_cons) return;

  {
    const int y_off = WS_Y + ((b * 56 + ci) * 128) * 64 + t * 16;
    u32x4 yg;
    ld_sc1(yg, y_off, rws);
    const bool zlive = t < 448;
    u32x4 zg = vs.zg;
    unsigned spins = 0;
    for (;;) {
      const bool ok = !zlive || ((zg[0] & 0xff000000u) == g0tag && zg[1] == g1tag && (zg[2] & 0xff000000u) == g0tag && zg[3] == g1tag);
      if (__all(ok) || ++spins >= SPIN_MAX) break;
      __builtin_amdgcn_s_sleep(2);
      if (!ok) ld_sc1_re(zg, z_off, rws);
    }
    float z0 = zlive ? __uint_as_float(zg[0] << 8) : 0.f, z1 = zlive ? __uint_as_float(zg[2] << 8) : 0.f;
    z0 += dpp<ROR4>(z0); z0 += dpp<XOR8>(z0); z0 = x16_sum(z0); z0 = x32_sum(z0);
    z1 += dpp<ROR4>(z1); z1 += dpp<XOR8>(z1); z1 = x16_sum(z1); z1 = x32_sum(z1);
    PIN1(z0); PIN1(z1);
    waitv<0>(yg);
    spins = 0;
    for (;;) {
      const bool ok = (yg[0] & 0xff000000u) == g0tag && yg[1] == g1tag && (yg[2] & 0xff000000u) == g0tag && yg[3] == g1tag;
      if (__all(ok) || ++spins >= SPIN_MAX) break;
      __builtin_amdgcn_s_sleep(2);
      if (!ok) ld_sc1_re(yg, y_off, rws);
    }
    float y0 = __uint_as_float(yg[0] << 8), y1 = __uint_as_float(yg[2] << 8);
    y0 += dpp<ROR4>(y0); y0 += dpp<XOR8>(y0); y0 = x16_sum(y0); y0 = x32_sum(y0);
    y1 += dpp<ROR4>(y1); y1 += dpp<XOR8>(y1); y1 = x16_sum(y1); y1 = x32_sum(y1);
    if (lane < 4) {
      *reinterpret_cast<f32x2*>(lds + L_C + w * 16 + lane * 2) = f32x2{y0, y1};
      *reinterpret_cast<f32x2*>(lds + L_C + w * 16 + 8 + lane * 2) = f32x2{z0, z1};
    }
    bar();
    if (t < 8) {
      float a = 0.f, f = 0.f;
#pragma unroll
      for (int ww = 0; ww < 8; ++ww) { a += lds[L_C + ww * 16 + t]; f += lds[L_C + ww * 16 + 8 + t]; }
      const unsigned xo = reinterpret_cast<const u16*>(ldsu + L_X)[ci * 8 + t];
      const float xr = __uint_as_float(xo << 16);
      const float res = rbf(xr + rbf(a)) + rbf(f);
      out[b * D_ + ci * 8 + t] = __builtin_bit_cast(u16, (__bf16)res);
    }
    if (ci == 0 && t == 0) {
      u32x2 ne = {tlo, thi_full};
      __builtin_amdgcn_raw_buffer_store_b64(ne, rs, WS_EPOCH + b * 256, 0, 16);
    }
  }
}

extern "C" void kernel_launch(void* const* d_in, const int* in_sizes, int n_in,
                              void* d_out, int out_size, void* d_ws, size_t ws_size,
                              hipStream_t stream) {
  const u16* x = (const u16*)d_in[0];
  const u16* k = (const u16*)d_in[1];
  const u16* v = (const u16*)d_in[2];
  const u16* wq = (const u16*)d_in[3];
  const u16* wo = (const u16*)d_in[4];
  const u16* wg = (const u16*)d_in[6];
  const u16* wu = (const u16*)d_in[7];
  const u16* wd = (const u16*)d_in[8];
  fused_kernel<<<NWG, NTHR, 0, stream>>>(x, k, v, wq, wo, wg, wu, wd, (u16*)d_out, (unsigned char*)d_ws);
}
```
